```python
import torch

def _softmax(x, axis=-1):
    return torch.softmax(x, dim=axis)

def _gelu(x):
    return torch.nn.functional.gelu(x, approximate='tanh')

def _silu(x):
    return torch.nn.functional.silu(x)

def _sigmoid(x):
    return torch.sigmoid(x)

def _layernorm(x, axis=-1, eps=1e-05):
    m = x.mean(dim=axis, keepdim=True)
    v = x.var(dim=axis, keepdim=True, unbiased=False)
    return (x - m) / torch.sqrt(v + eps)

def _scatter_add(tgt, idx, src, axis=0):
    return tgt.clone().index_add_(axis, idx, src)

def _segment_sum(src, seg, n):
    out = torch.zeros((n,) + src.shape[1:], dtype=src.dtype)
    return out.index_add_(0, seg, src)

def _pad(x, pw):
    flat = tuple((v for pair in reversed(pw) for v in pair))
    return torch.nn.functional.pad(x, flat)
kernel_name = 'standalone__kv_cache_quant_pre_rope_vs_post_rope__prompt'

def setup_inputs():

    _w = lambda a: (lambda t: t.to(torch.bfloat16) if t.is_floating_point() else t)(torch.as_tensor(a))
    B, Sq, Skv = (1, 2048, 2048)
    H, Dh0 = (8, 128)
    G = -1 if -1 > 0 else Dh0
    G = max(2, G // 2 * 2)
    Dh = max(G, Dh0 // G * G)
    Skv = max(Skv, Sq)
    ng = Dh // G
    bits = 2 if 2 > 1 else 4
    bits = max(2, min(8, bits))
    qmax = (1 << bits) - 1
    half = Dh // 2
    inv = 1.0 / 10000.0 ** (torch.arange(half) / max(1, half))
    ang_k = torch.arange(Skv)[:, None] * inv[None, :]
    ang_q = torch.arange(Skv - Sq, Skv)[:, None] * inv[None, :]
    kc = torch.cat([torch.cos(ang_k), torch.cos(ang_k)], dim=-1)
    ks = torch.cat([torch.sin(ang_k), torch.sin(ang_k)], dim=-1)
    qc = torch.cat([torch.cos(ang_q), torch.cos(ang_q)], dim=-1)
    qs = torch.cat([torch.sin(ang_q), torch.sin(ang_q)], dim=-1)
    qk_i = torch.randint(0, qmax + 1, (B, H, Skv, Dh)).to(torch.int32)
    qv_i = torch.randint(0, qmax + 1, (B, H, Skv, Dh)).to(torch.int32)
    ksc = torch.abs((lambda *s: torch.randn(*s, dtype=torch.bfloat16) * 0.4)(B, H, Skv, ng)) * 0.1 + 0.01
    kzp = torch.full((B, H, Skv, ng), qmax / 2.0)
    vsc = torch.abs((lambda *s: torch.randn(*s, dtype=torch.bfloat16) * 0.4)(B, H, Skv, ng)) * 0.1 + 0.01
    vzp = torch.full((B, H, Skv, ng), qmax / 2.0)
    return {'q': _w((lambda *s: torch.randn(*s, dtype=torch.bfloat16) * 6.4)(B, H, Sq, Dh) * 0.5), 'qk': _w(qk_i), 'k_scales': _w(ksc), 'k_zeros': _w(kzp), 'qv': _w(qv_i), 'v_scales': _w(vsc), 'v_zeros': _w(vzp), 'q_cos': _w(qc), 'q_sin': _w(qs), 'k_cos': _w(kc), 'k_sin': _w(ks)}

def reference(q, qk, k_scales, k_zeros, qv, v_scales, v_zeros, q_cos, q_sin, k_cos, k_sin):
    B, H, Sq, Dh = q.shape
    ng = k_scales.shape[-1]
    G = Dh // ng
    half = Dh // 2
    ks = (lambda x, n, axis: torch.repeat_interleave(x, n, dim=axis))(k_scales, G, axis=-1)
    kz = (lambda x, n, axis: torch.repeat_interleave(x, n, dim=axis))(k_zeros, G, axis=-1)
    k = ((lambda x, dt: x.to(dt))(qk, q.dtype) - kz) * ks
    vs = (lambda x, n, axis: torch.repeat_interleave(x, n, dim=axis))(v_scales, G, axis=-1)
    vz = (lambda x, n, axis: torch.repeat_interleave(x, n, dim=axis))(v_zeros, G, axis=-1)
    v = ((lambda x, dt: x.to(dt))(qv, q.dtype) - vz) * vs
    k1 = k[..., :half].clone()
    k2 = k[..., half:].clone()
    krh = torch.concatenate([-k2, k1], axis=-1)
    kr = k * k_cos[None, None, :, :] + krh * k_sin[None, None, :, :]
    q1 = q[..., :half].clone()
    q2 = q[..., half:].clone()
    qrh = torch.concatenate([-q2, q1], axis=-1)
    qr = q * q_cos[None, None, :, :] + qrh * q_sin[None, None, :, :]
    logits = torch.einsum('bhqd,bhkd->bhqk', qr, kr) * float(Dh) ** (-0.5)
    attn = _softmax(logits, axis=-1)
    out = torch.einsum('bhqk,bhkd->bhqd', attn, v)
    return out
```

```cpp
#include <hip/hip_runtime.h>
#include <hip/hip_bf16.h>
#include <stdint.h>

typedef float    f32x16 __attribute__((ext_vector_type(16)));
typedef float    f32x4  __attribute__((ext_vector_type(4)));
typedef float    f32x2  __attribute__((ext_vector_type(2)));
typedef __bf16   bf16x8 __attribute__((ext_vector_type(8)));
typedef __bf16   bf16x2 __attribute__((ext_vector_type(2)));
typedef unsigned u32x4  __attribute__((ext_vector_type(4)));
typedef int      i32x4  __attribute__((ext_vector_type(4)));
#define DI __device__ __forceinline__

constexpr int H = 8, S = 2048, D = 128;
constexpr float QSCALE = 0.12751743082f;

DI unsigned cvtpk(float lo, float hi) { f32x2 v = {lo, hi}; bf16x2 b = __builtin_convertvector(v, bf16x2); return __builtin_bit_cast(unsigned, b); }
DI float bflo(unsigned w) { return __uint_as_float(w << 16); }
DI float bfhi(unsigned w) { return __uint_as_float(w & 0xffff0000u); }
DI float bf1(const unsigned short* p) { return __uint_as_float((unsigned)(*p) << 16); }

DI void rope8(const float* x1, const float* x2, u32x4 c1, u32x4 s1, u32x4 c2, u32x4 s2, float scale, u32x4& o1, u32x4& o2) {
#pragma unroll
  for (int e = 0; e < 4; ++e) {
    float a0 = (x1[2*e]   * bflo(c1[e]) - x2[2*e]   * bflo(s1[e])) * scale;
    float a1 = (x1[2*e+1] * bfhi(c1[e]) - x2[2*e+1] * bfhi(s1[e])) * scale;
    float b0 = (x2[2*e]   * bflo(c2[e]) + x1[2*e]   * bflo(s2[e])) * scale;
    float b1 = (x2[2*e+1] * bfhi(c2[e]) + x1[2*e+1] * bfhi(s2[e])) * scale;
    o1[e] = cvtpk(a0, a1); o2[e] = cvtpk(b0, b1);
  }
}

__global__ __launch_bounds__(256) void prep_kernel(const int* __restrict__ qk, const unsigned short* __restrict__ ksc, const unsigned short* __restrict__ kzp,
                                                   const int* __restrict__ qv, const unsigned short* __restrict__ vsc, const unsigned short* __restrict__ vzp,
                                                   const unsigned short* __restrict__ kcos, const unsigned short* __restrict__ ksin,
                                                   char* __restrict__ Kf, char* __restrict__ Vf) {
  const int b = blockIdx.x, h = b & 7, t = b >> 3, tid = threadIdx.x, w = tid >> 6, lane = tid & 63;
  const int key0 = t * 64 + w * 16;
  {
    const int rl = lane >> 3, c = lane & 7;
#pragma unroll
    for (int p = 0; p < 2; ++p) {
      const int key = key0 + p * 8 + rl;
      const int* src = qk + ((size_t)h * S + key) * D + 8 * c;
      i32x4 a0 = *(const i32x4*)src, a1 = *(const i32x4*)(src + 4), b0 = *(const i32x4*)(src + 64), b1 = *(const i32x4*)(src + 68);
      u32x4 c1 = *(const u32x4*)(kcos + key * D + 8 * c), c2 = *(const u32x4*)(kcos + key * D + 64 + 8 * c);
      u32x4 s1 = *(const u32x4*)(ksin + key * D + 8 * c), s2 = *(const u32x4*)(ksin + key * D + 64 + 8 * c);
      const float sc = bf1(ksc + h * S + key), zp = bf1(kzp + h * S + key);
      float x1[8], x2[8];
#pragma unroll
      for (int e = 0; e < 4; ++e) { x1[e] = (float)a0[e] - zp; x1[4+e] = (float)a1[e] - zp; x2[e] = (float)b0[e] - zp; x2[4+e] = (float)b1[e] - zp; }
      u32x4 o1, o2; rope8(x1, x2, c1, s1, c2, s2, sc, o1, o2);
      char* dst = Kf + ((size_t)h * 64 + (key >> 5)) * 8192 + (c >> 1) * 1024 + (c & 1) * 512 + (key & 31) * 16;
      *(u32x4*)dst = o1; *(u32x4*)(dst + 4096) = o2;
    }
  }
  {
    const int dl = lane & 31, hh = lane >> 5;
    float x[8][4];
#pragma unroll
    for (int j = 0; j < 8; ++j) {
      const int key = key0 + 8 * (j >> 2) + 4 * hh + (j & 3);
      i32x4 a = *(const i32x4*)(qv + ((size_t)h * S + key) * D + 4 * dl);
      const float sc = bf1(vsc + h * S + key), zp = bf1(vzp + h * S + key);
#pragma unroll
      for (int e = 0; e < 4; ++e) x[j][e] = ((float)a[e] - zp) * sc;
    }
    char* dst = Vf + ((size_t)h * 128 + (key0 >> 4)) * 4096 + lane * 16;
#pragma unroll
    for (int db = 0; db < 4; ++db) {
      u32x4 o = {cvtpk(x[0][db], x[1][db]), cvtpk(x[2][db], x[3][db]), cvtpk(x[4][db], x[5][db]), cvtpk(x[6][db], x[7][db])};
      *(u32x4*)(dst + db * 1024) = o;
    }
  }
}

#define MFMA(a, b, c) __builtin_amdgcn_mfma_f32_32x32x16_bf16((a), (b), (c), 0, 0, 0)

__global__ __launch_bounds__(256, 1) void attn_kernel(const unsigned short* __restrict__ q, const unsigned short* __restrict__ qcos, const unsigned short* __restrict__ qsin,
                                                      const char* __restrict__ Kf, const char* __restrict__ Vf, unsigned short* __restrict__ out) {
  extern __shared__ __attribute__((aligned(16))) char lds[];
  const int b = blockIdx.x, h = b & 7, t = b >> 3, tid = threadIdx.x, w = tid >> 6, lane = tid & 63;
  const int hh = lane >> 5;
  {
    const int rl = lane >> 3, c = lane & 7;
#pragma unroll
    for (int p = 0; p < 2; ++p) {
      const int rloc = w * 16 + p * 8 + rl, row = t * 64 + rloc;
      const unsigned short* src = q + ((size_t)h * S + row) * D + 8 * c;
      u32x4 a = *(const u32x4*)src, bb = *(const u32x4*)(src + 64);
      u32x4 c1 = *(const u32x4*)(qcos + row * D + 8 * c), c2 = *(const u32x4*)(qcos + row * D + 64 + 8 * c);
      u32x4 s1 = *(const u32x4*)(qsin + row * D + 8 * c), s2 = *(const u32x4*)(qsin + row * D + 64 + 8 * c);
      float x1[8], x2[8];
#pragma unroll
      for (int e = 0; e < 4; ++e) { x1[2*e] = bflo(a[e]); x1[2*e+1] = bfhi(a[e]); x2[2*e] = bflo(bb[e]); x2[2*e+1] = bfhi(bb[e]); }
      u32x4 o1, o2; rope8(x1, x2, c1, s1, c2, s2, QSCALE, o1, o2);
      char* dst = lds + (rloc >> 5) * 8192 + (c >> 1) * 1024 + (c & 1) * 512 + (rloc & 31) * 16;
      *(u32x4*)dst = o1; *(u32x4*)(dst + 4096) = o2;
    }
  }
  __syncthreads();
  bf16x8 Q[2][8];
#pragma unroll
  for (int qb = 0; qb < 2; ++qb)
#pragma unroll
    for (int d0 = 0; d0 < 8; ++d0) Q[qb][d0] = *(const bf16x8*)(lds + qb * 8192 + d0 * 1024 + lane * 16);
  __syncthreads();
  f32x16 O[4][2];
#pragma unroll
  for (int i = 0; i < 4; ++i) { O[i][0] = f32x16{}; O[i][1] = f32x16{}; }
  float m[2] = {-1e30f, -1e30f}, l[2] = {0.f, 0.f};
  const char* Kw = Kf + ((size_t)h * 64 + w * 16) * 8192 + lane * 16;
  const char* Vw = Vf + ((size_t)h * 64 + w * 16) * 8192 + lane * 16;
  for (int st = 0; st < 16; ++st) {
    f32x16 Sx[2] = {f32x16{}, f32x16{}};
#pragma unroll
    for (int d0 = 0; d0 < 8; ++d0) {
      bf16x8 kf = *(const bf16x8*)(Kw + st * 8192 + d0 * 1024);
      Sx[0] = MFMA(kf, Q[0][d0], Sx[0]); Sx[1] = MFMA(kf, Q[1][d0], Sx[1]);
    }
    bf16x8 P[2][2];
#pragma unroll
    for (int qb = 0; qb < 2; ++qb) {
      float mx = Sx[qb][0];
#pragma unroll
      for (int r = 1; r < 16; ++r) mx = fmaxf(mx, Sx[qb][r]);
      mx = fmaxf(mx, __shfl_xor(mx, 32));
      const float mn = fmaxf(m[qb], mx), al = __builtin_amdgcn_exp2f(m[qb] - mn);
      m[qb] = mn; float ps = 0.f; float p[16];
#pragma unroll
      for (int r = 0; r < 16; ++r) { p[r] = __builtin_amdgcn_exp2f(Sx[qb][r] - mn); ps += p[r]; }
      l[qb] = l[qb] * al + ps;
#pragma unroll
      for (int db = 0; db < 4; ++db) O[db][qb] *= al;
#pragma unroll
      for (int s = 0; s < 2; ++s) { u32x4 pk = {cvtpk(p[8*s], p[8*s+1]), cvtpk(p[8*s+2], p[8*s+3]), cvtpk(p[8*s+4], p[8*s+5]), cvtpk(p[8*s+6], p[8*s+7])}; P[qb][s] = __builtin_bit_cast(bf16x8, pk); }
    }
#pragma unroll
    for (int s = 0; s < 2; ++s)
#pragma unroll
      for (int db = 0; db < 4; ++db) {
        bf16x8 vf = *(const bf16x8*)(Vw + st * 8192 + s * 4096 + db * 1024);
        O[db][0] = MFMA(vf, P[0][s], O[db][0]); O[db][1] = MFMA(vf, P[1][s], O[db][1]);
      }
  }
  float* ml = (float*)(lds + 131072);
#pragma unroll
  for (int qb = 0; qb < 2; ++qb) { l[qb] += __shfl_xor(l[qb], 32); if (hh == 0) { ml[((w * 2 + qb) * 2 + 0) * 32 + lane] = m[qb]; ml[((w * 2 + qb) * 2 + 1) * 32 + lane] = l[qb]; } }
  __syncthreads();
  float f[2];
#pragma unroll
  for (int qb = 0; qb < 2; ++qb) {
    float mm = -1e30f;
#pragma unroll
    for (int u = 0; u < 4; ++u) mm = fmaxf(mm, ml[((u * 2 + qb) * 2 + 0) * 32 + (lane & 31)]);
    float ll = 0.f;
#pragma unroll
    for (int u = 0; u < 4; ++u) ll += ml[((u * 2 + qb) * 2 + 1) * 32 + (lane & 31)] * __builtin_amdgcn_exp2f(ml[((u * 2 + qb) * 2 + 0) * 32 + (lane & 31)] - mm);
    f[qb] = __builtin_amdgcn_exp2f(m[qb] - mm) / ll;
  }
#pragma unroll
  for (int g = 0; g < 4; ++g)
#pragma unroll
    for (int db = 0; db < 4; ++db)
#pragma unroll
      for (int qb = 0; qb < 2; ++qb) {
        f32x4 v = {O[db][qb][4*g] * f[qb], O[db][qb][4*g+1] * f[qb], O[db][qb][4*g+2] * f[qb], O[db][qb][4*g+3] * f[qb]};
        *(f32x4*)(lds + (((g * 4 + w) * 4 + db) * 2 + qb) * 1024 + lane * 16) = v;
      }
  __syncthreads();
#pragma unroll
  for (int qb = 0; qb < 2; ++qb) {
    f32x4 acc[4];
#pragma unroll
    for (int db = 0; db < 4; ++db) {
      acc[db] = f32x4{};
#pragma unroll
      for (int u = 0; u < 4; ++u) acc[db] += *(const f32x4*)(lds + (((w * 4 + u) * 4 + db) * 2 + qb) * 1024 + lane * 16);
    }
    u32x4 o0 = {cvtpk(acc[0][0], acc[1][0]), cvtpk(acc[2][0], acc[3][0]), cvtpk(acc[0][1], acc[1][1]), cvtpk(acc[2][1], acc[3][1])};
    u32x4 o1 = {cvtpk(acc[0][2], acc[1][2]), cvtpk(acc[2][2], acc[3][2]), cvtpk(acc[0][3], acc[1][3]), cvtpk(acc[2][3], acc[3][3])};
    unsigned short* dst = out + ((size_t)h * S + t * 64 + qb * 32 + (lane & 31)) * D + 32 * w + 16 * hh;
    *(u32x4*)dst = o0; *(u32x4*)(dst + 8) = o1;
  }
}

extern "C" void kernel_launch(void* const* d_in, const int* in_sizes, int n_in, void* d_out, int out_size, void* d_ws, size_t ws_size, hipStream_t stream) {
  char* Kf = (char*)d_ws; char* Vf = Kf + (size_t)H * S * D * 2;
  const int LDS = 131072 + 2048;
  hipFuncSetAttribute((const void*)attn_kernel, hipFuncAttributeMaxDynamicSharedMemorySize, LDS);
  prep_kernel<<<256, 256, 0, stream>>>((const int*)d_in[1], (const unsigned short*)d_in[2], (const unsigned short*)d_in[3],
                                       (const int*)d_in[4], (const unsigned short*)d_in[5], (const unsigned short*)d_in[6],
                                       (const unsigned short*)d_in[9], (const unsigned short*)d_in[10], Kf, Vf);
  attn_kernel<<<256, 256, LDS, stream>>>((const unsigned short*)d_in[0], (const unsigned short*)d_in[7], (const unsigned short*)d_in[8], Kf, Vf, (unsigned short*)d_out);
}
```

```cpp
#include <hip/hip_runtime.h>
#include <stdint.h>

constexpr int LDS_TOTAL = 139328;
constexpr uint64_t WS_BYTES = 0xC80000;
typedef const void* P;
typedef uint64_t U;

__global__ __launch_bounds__(256, 1) void fused_kernel(void* out, U n0, void* wsv, U n1, P qk, U n2, P qv, U n3, P kcos, U n4, P ksin, U n5, P ks, U n6, P kz, U n7,
                                                       P vs, U n8, P vz, U n9, P q, U n10, P qcos, U n11, P qsin, U n12, void* ws, U n13) {
  const unsigned w = __builtin_amdgcn_readfirstlane(threadIdx.x >> 6);
  const uint64_t karg = (uint64_t)__builtin_amdgcn_kernarg_segment_ptr();
  asm volatile(
    "s_load_dwordx16 s[12:27], %[karg], 32\n"
    "s_load_dwordx16 s[28:43], %[karg], 96\n"
    "s_load_dwordx16 s[44:59], %[karg], 160\n"
    "s_load_dwordx8 s[4:11], %[karg], 0\n"
    "v_mbcnt_lo_u32_b32 v25, -1, 0\n"
    "v_mbcnt_hi_u32_b32 v25, -1, v25\n"
    "s_lshr_b32 s88, %[bid], 3\n"
    "s_lshl2_add_u32 s88, s88, %[w]\n"
    "s_cmp_lt_u32 s88, 4\n"
    "s_cbranch_scc0 .Lf_NOPF\n"
    "s_getpc_b64 s[86:87]\n"
    "s_lshl_b32 s88, s88, 13\n"
    "v_lshlrev_b32 v7, 7, v25\n"
    "v_add_u32 v7, s88, v7\n"
    "global_load_dword v7, v7, s[86:87]\n"
    ".Lf_NOPF:\n"
    "s_and_b32 s92, %[bid], 7\n"
    "s_lshr_b32 s94, %[bid], 5\n"
    "s_lshl3_add_u32 s94, s94, s92\n"
    "s_bfe_u32 s95, %[bid], 0x20003\n"
    "s_lshr_b32 s83, %[w], 1\n"
    "s_lshl1_add_u32 s83, s95, s83\n"
    "s_lshr_b32 s61, s94, 2\n"
    "s_and_b32 s89, s94, 3\n"
    "s_lshl_b32 s62, s83, 6\n"
    "s_add_u32 s62, s62, s61\n"
    "s_lshl_b32 s60, s62, 14\n"
    "s_lshl_b32 s62, s62, 6\n"
    "s_lshl_b32 s61, s61, 13\n"
    "s_and_b32 s85, %[w], 1\n"
    "s_lshl_b32 s85, s85, 7\n"
    "v_lshrrev_b32 v2, 3, v25\n"
    "v_lshlrev_b32 v0, 4, v25\n"
    "v_and_b32 v5, 7, v25\n"
    "s_lshl_b32 s88, s89, 12\n"
    "s_add_u32 s88, s88, s85\n"
    "v_lshl_add_u32 v19, v2, 9, s88\n"
    "v_lshl_add_u32 v19, v5, 4, v19\n"
    "s_lshl_b32 s88, s89, 3\n"
    "v_and_b32 v61, 16, v25\n"
    "v_lshrrev_b32 v61, 2, v61\n"
    "v_and_or_b32 v61, v25, 32, v61\n"
    "v_add_u32 v61, s88, v61\n"
    "s_lshl_b32 s88, s85, 1\n"
    "v_lshl_add_u32 v60, v5, 5, s88\n"
    "v_lshl_add_u32 v60, v61, 8, v60\n"
    "v_and_b32 v4, 8, v25\n"
    "v_lshl_add_u32 v60, v4, 1, v60\n"
    "v_lshrrev_b32 v20, 1, v19\n"
    "s_lshl_b32 s88, s89, 4\n"
    "v_lshl_add_u32 v21, v2, 1, s88\n"
    "v_mov_b32 v26, 0\n"
    "v_mov_b32 v27, 0\n"
    "s_lshr_b32 s93, %[bid], 4\n"
    "s_and_b32 s88, s92, 1\n"
    "s_lshl_b32 s88, s88, 4\n"
    "s_add_u32 s93, s93, s88\n"
    "s_and_b32 s92, s92, 6\n"
    "s_bfe_u32 s88, %[bid], 0x10003\n"
    "s_or_b32 s92, s92, s88\n"
    "v_lshl_add_u32 v3, v2, 7, v0\n"
    "s_lshl_b32 s84, s93, 14\n"
    "s_lshl_b32 s88, %[w], 12\n"
    "s_add_u32 s84, s84, s88\n"
    "s_lshl_b32 s82, s92, 19\n"
    "s_add_u32 s82, s82, s84\n"
    "s_lshr_b32 s80, s94, 2\n"
    "s_lshl_b32 s80, s80, 13\n"
    "s_lshl_b32 s81, s83, 20\n"
    "s_add_u32 s80, s80, s81\n"
    "s_add_u32 s80, s80, 65536\n"
    "s_lshr_b32 s88, s89, 1\n"
    "s_lshl_b32 s88, s88, 12\n"
    "s_lshl3_add_u32 s88, s85, s88\n"
    "s_and_b32 s81, s89, 1\n"
    "s_lshl_b32 s81, s81, 7\n"
    "s_add_u32 s88, s88, s81\n"
    "v_and_b32 v30, 0x37, v25\n"
    "v_lshl_add_u32 v30, v30, 4, s88\n"
    "v_lshl_add_u32 v30, v4, 8, v30\n"
    "s_lshl_b32 s88, s89, 8\n"
    "s_add_u32 s88, s88, s85\n"
    "s_add_u32 s88, s88, 0x80000\n"
    "v_lshl_add_u32 v58, v5, 4, s88\n"
    "v_lshl_add_u32 v58, v4, 9, v58\n"
    "v_and_b32 v28, 48, v25\n"
    "v_lshl_add_u32 v58, v28, 6, v58\n"
    "s_lshl_b32 s81, s95, 6\n"
    "s_bfe_u32 s88, s94, 0x20002\n"
    "s_lshl4_add_u32 s81, s88, s81\n"
    "s_lshr_b32 s88, s94, 4\n"
    "s_lshl2_add_u32 s88, s88, s89\n"
    "s_add_u32 s81, s81, s88\n"
    "s_lshl_b32 s81, s81, 7\n"
    "s_mov_b32 s69, 0x820000\n"
    "s_add_u32 s81, s81, s69\n"
    "s_mov_b32 s78, 0x05040100\n"
    "s_mov_b32 s79, 0x07060302\n"
    "s_mul_i32 s88, %[w], 2304\n"
    "s_add_u32 s88, s88, 115712\n"
    "v_mul_u32_u24 v28, 144, v5\n"
    "v_lshl_add_u32 v28, v2, 4, v28\n"
    "v_add_u32 v28, s88, v28\n"
    "v_mul_u32_u24 v29, 144, v2\n"
    "v_lshl_add_u32 v29, v5, 4, v29\n"
    "v_add_u32 v29, s88, v29\n"
    "v_mov_b32 v32, 0\n"
    "v_mov_b32 v33, 0\n"
    "v_mov_b32 v34, 0\n"
    "v_mov_b32 v35, 0\n"
    "v_mov_b32 v6, 126976\n"
    "ds_write_b128 v6, v[32:35]\n"
    "ds_write_b32 v6, v32 offset:16\n"
    "v_add_u32 v6, 124928, v0\n"
    "ds_write_b128 v6, v[32:35]\n"
    "ds_write_b128 v6, v[32:35] offset:1024\n"
    "v_mul_u32_u24 v18, 272, v5\n"
    "v_lshl_add_u32 v18, v2, 4, v18\n"
    "s_mul_i32 s88, %[w], 4352\n"
    "s_add_u32 s88, s88, 98304\n"
    "v_add_u32 v18, s88, v18\n"
    "s_lshl_b32 s72, s92, 20\n"
    "s_lshl_b32 s88, %[w], 13\n"
    "s_add_u32 s72, s72, s88\n"
    "s_add_u32 s72, s72, 65536\n"
    "s_mov_b32 s73, 0\n"
    "s_mov_b32 s90, 200000\n"
    "s_mov_b32 s91, 0xc1f00000\n"
    "s_mul_i32 s64, %[w], 3072\n"
    "s_add_u32 s64, s64, 127040\n"
    "v_add_u32 v1, 0x1000, v0\n"
    "s_lshr_b32 s88, s92, 1\n"
    "s_lshl_b32 s88, s88, 2\n"
    "s_add_u32 s88, s88, %[w]\n"
    "s_lshl_b32 s88, s88, 11\n"
    "s_add_u32 s88, s88, s69\n"
    "s_and_b32 s89, s93, 0\n"
    "s_mul_i32 s89, s89, 0x20000\n"
    "s_add_u32 s88, s88, s89\n"
    "v_and_b32 v24, 15, v25\n"
    "v_lshlrev_b32 v24, 7, v24\n"
    "v_add_u32 v24, s88, v24\n"
    "v_lshrrev_b32 v116, 2, v25\n"
    "v_and_b32 v117, 3, v25\n"
    "v_lshlrev_b32 v116, 8, v116\n"
    "v_lshl_add_u32 v116, v117, 3, v116\n"
    "s_lshl_b32 s92, s92, 12\n"
    "s_lshl_b32 s88, %[w], 6\n"
    "s_add_u32 s92, s92, s88\n"
    "v_mad_u32_u24 v117, v25, 48, s64\n"
    "v_lshrrev_b32 v22, 4, v25\n"
    "v_lshl_add_u32 v22, v22, 4, v0\n"
    "s_add_u32 s88, %[w], 1\n"
    "s_and_b32 s88, s88, 3\n"
    "s_mul_i32 s88, s88, 4352\n"
    "s_add_u32 s88, s88, 98304\n"
    "v_add_u32 v10, s88, v22\n"
    "s_add_u32 s88, %[w], 2\n"
    "s_and_b32 s88, s88, 3\n"
    "s_mul_i32 s88, s88, 4352\n"
    "s_add_u32 s88, s88, 98304\n"
    "v_add_u32 v11, s88, v22\n"
    "s_add_u32 s88, %[w], 3\n"
    "s_and_b32 s88, s88, 3\n"
    "s_mul_i32 s88, s88, 4352\n"
    "s_add_u32 s88, s88, 98304\n"
    "v_add_u32 v12, s88, v22\n"
    "s_add_u32 s88, %[w], 4\n"
    "s_and_b32 s88, s88, 3\n"
    "s_mul_i32 s88, s88, 4352\n"
    "s_add_u32 s88, s88, 98304\n"
    "v_add_u32 v13, s88, v22\n"
    "s_waitcnt lgkmcnt(0)\n"
    "buffer_load_dwordx4 v[224:227], v19, s[12:15], s60 offen offset:0 nt\n"
    "buffer_load_dwordx4 v[228:231], v19, s[12:15], s60 offen offset:256 nt\n"
    "buffer_load_dwordx4 v[232:235], v60, s[16:19], s60 offen offset:0 nt\n"
    "buffer_load_dwordx4 v[236:239], v60, s[16:19], s60 offen offset:512 nt\n"
    "buffer_load_dwordx2 v[48:49], v20, s[20:23], s61 offen offset:0\n"
    "buffer_load_dwordx2 v[50:51], v20, s[20:23], s61 offen offset:128\n"
    "buffer_load_dwordx2 v[52:53], v20, s[24:27], s61 offen offset:0\n"
    "buffer_load_dwordx2 v[54:55], v20, s[24:27], s61 offen offset:128\n"
    "buffer_load_short_d16_hi v26, v21, s[32:35], s62 offen\n"
    "buffer_load_short_d16_hi v27, v21, s[28:31], s62 offen\n"
    "buffer_load_dword v56, v61, s[36:39], s62 offen\n"
    "buffer_load_dword v57, v61, s[40:43], s62 offen\n"
    "buffer_load_dwordx2 v[8:9], off, s[56:59], 0 sc1\n"
    "s_add_u32 s60, s60, 0x40000\n"
    "s_add_u32 s61, s61, 0x20000\n"
    "s_add_u32 s62, s62, 0x400\n"
    "buffer_load_dwordx4 v[64:67], v3, s[44:47], s82 offen offset:0 nt\n"
    "buffer_load_dwordx4 v[68:71], v3, s[44:47], s82 offen offset:128 nt\n"
    "buffer_load_dwordx4 v[72:75], v3, s[48:51], s84 offen offset:0\n"
    "buffer_load_dwordx4 v[76:79], v3, s[48:51], s84 offen offset:128\n"
    "buffer_load_dwordx4 v[80:83], v3, s[52:55], s84 offen offset:0\n"
    "buffer_load_dwordx4 v[84:87], v3, s[52:55], s84 offen offset:128\n"
    "buffer_load_dwordx2 v[112:113], v116, s[36:39], s92 offen offset:0\n"
    "buffer_load_dwordx2 v[114:115], v116, s[36:39], s92 offen offset:32\n"
    "s_add_u32 s94, %[w], 1\n"
    "s_and_b32 s94, s94, 3\n"
    "s_mul_i32 s63, s94, 24576\n"
    "s_add_u32 s63, s63, 0\n"
    "s_lshl_b32 s94, s94, 9\n"
    "s_add_u32 s68, s94, 124928\n"
    "s_add_u32 s94, %[w], 2\n"
    "s_and_b32 s94, s94, 3\n"
    "s_mul_i32 s66, s94, 24576\n"
    "s_add_u32 s66, s66, 8192\n"
    "s_lshl_b32 s94, s94, 9\n"
    "s_add_u32 s70, s94, 125056\n"
    "s_add_u32 s94, %[w], 3\n"
    "s_and_b32 s94, s94, 3\n"
    "s_mul_i32 s67, s94, 24576\n"
    "s_add_u32 s67, s67, 16384\n"
    "s_lshl_b32 s94, s94, 9\n"
    "s_add_u32 s71, s94, 125184\n"
    "s_add_u32 s94, %[w], 4\n"
    "s_and_b32 s94, s94, 3\n"
    "s_lshl_b32 s94, s94, 9\n"
    "s_add_u32 s86, s94, 125312\n"
    "s_mov_b32 s83, 0x39803980\n"
    "s_mov_b32 s93, 0x45804580\n"
    "s_mul_i32 s87, %[w], 24576\n"
    "s_add_u32 s95, s94, 124928\n"
    "s_mov_b32 s94, s82\n"
    "s_waitcnt vmcnt(9)\n"
    "s_call_b64 s[76:77], .Lf_PROC1\n"
    "s_branch .Lf_PROCX1\n"
    ".p2align 3\n"
    ".Lf_PROC1:\n"
    "v_cvt_f32_i32 v224, v224\n"
    "v_cvt_f32_i32 v225, v225\n"
    "v_cvt_f32_i32 v226, v226\n"
    "v_cvt_f32_i32 v227, v227\n"
    "v_cvt_f32_i32 v228, v228\n"
    "v_cvt_f32_i32 v229, v229\n"
    "v_cvt_f32_i32 v230, v230\n"
    "v_cvt_f32_i32 v231, v231\n"
    "v_mul_f32 v27, 0x3e0293ee, v27\n"
    "v_pk_add_f32 v[224:225], v[224:225], v[26:27] op_sel_hi:[1,0] neg_lo:[0,1] neg_hi:[0,1]\n"
    "v_pk_add_f32 v[226:227], v[226:227], v[26:27] op_sel_hi:[1,0] neg_lo:[0,1] neg_hi:[0,1]\n"
    "v_pk_add_f32 v[228:229], v[228:229], v[26:27] op_sel_hi:[1,0] neg_lo:[0,1] neg_hi:[0,1]\n"
    "v_pk_add_f32 v[230:231], v[230:231], v[26:27] op_sel_hi:[1,0] neg_lo:[0,1] neg_hi:[0,1]\n"
    "v_cvt_pk_bf16_f32 v224, v224, v228\n"
    "v_cvt_pk_bf16_f32 v225, v225, v229\n"
    "v_cvt_pk_bf16_f32 v226, v226, v230\n"
    "v_cvt_pk_bf16_f32 v227, v227, v231\n"
    "v_perm_b32 v244, v50, v54, s78\n"
    "v_perm_b32 v240, v52, v48, s78\n"
    "v_perm_b32 v245, v50, v54, s79\n"
    "v_perm_b32 v241, v52, v48, s79\n"
    "v_perm_b32 v246, v51, v55, s78\n"
    "v_perm_b32 v242, v53, v49, s78\n"
    "v_perm_b32 v247, v51, v55, s79\n"
    "v_perm_b32 v243, v53, v49, s79\n"
    "v_dot2_f32_bf16 v228, v224, v244, 0\n"
    "v_dot2_f32_bf16 v229, v225, v245, 0\n"
    "v_dot2_f32_bf16 v230, v226, v246, 0\n"
    "v_dot2_f32_bf16 v231, v227, v247, 0\n"
    "v_dot2_f32_bf16 v224, v224, v240, 0 neg_hi:[0,1,0]\n"
    "v_dot2_f32_bf16 v225, v225, v241, 0 neg_hi:[0,1,0]\n"
    "v_dot2_f32_bf16 v226, v226, v242, 0 neg_hi:[0,1,0]\n"
    "v_dot2_f32_bf16 v227, v227, v243, 0 neg_hi:[0,1,0]\n"
    "v_pk_mul_f32 v[228:229], v[228:229], v[26:27] op_sel:[0,1] op_sel_hi:[1,1]\n"
    "v_pk_mul_f32 v[230:231], v[230:231], v[26:27] op_sel:[0,1] op_sel_hi:[1,1]\n"
    "v_pk_mul_f32 v[224:225], v[224:225], v[26:27] op_sel:[0,1] op_sel_hi:[1,1]\n"
    "v_pk_mul_f32 v[226:227], v[226:227], v[26:27] op_sel:[0,1] op_sel_hi:[1,1]\n"
    "v_cvt_pk_bf16_f32 v242, v228, v229\n"
    "v_cvt_pk_bf16_f32 v243, v230, v231\n"
    "v_cvt_pk_bf16_f32 v240, v224, v225\n"
    "v_cvt_pk_bf16_f32 v241, v226, v227\n"
    "ds_write_b128 v28, v[240:243]\n"
    "ds_read_b128 v[248:251], v29\n"
    "v_and_b32 v244, 0x7fff7fff, v56\n"
    "v_pk_max_u16 v244, v244, s83\n"
    "v_pk_min_u16 v244, v244, s93\n"
    "v_sub_u32 v244, 0x3f803f80, v244\n"
    "v_add_u32_e64 v56, v56, v244\n"
    "v_lshlrev_b32 v244, 16, v56\n"
    "v_lshlrev_b32 v245, 16, v57\n"
    "v_and_b32 v246, 0xffff0000, v56\n"
    "v_and_b32 v247, 0xffff0000, v57\n"
    "v_cvt_f32_i32 v232, v232\n"
    "v_cvt_f32_i32 v233, v233\n"
    "v_cvt_f32_i32 v234, v234\n"
    "v_cvt_f32_i32 v235, v235\n"
    "v_cvt_f32_i32 v236, v236\n"
    "v_cvt_f32_i32 v237, v237\n"
    "v_cvt_f32_i32 v238, v238\n"
    "v_cvt_f32_i32 v239, v239\n"
    "v_mul_f32 v245, -v245, v244\n"
    "v_mul_f32 v247, -v247, v246\n"
    "v_pk_fma_f32 v[232:233], v[232:233], v[244:245], v[244:245] op_sel:[0,0,1] op_sel_hi:[1,0,1]\n"
    "v_pk_fma_f32 v[236:237], v[236:237], v[246:247], v[246:247] op_sel:[0,0,1] op_sel_hi:[1,0,1]\n"
    "v_pk_fma_f32 v[234:235], v[234:235], v[244:245], v[244:245] op_sel:[0,0,1] op_sel_hi:[1,0,1]\n"
    "v_pk_fma_f32 v[238:239], v[238:239], v[246:247], v[246:247] op_sel:[0,0,1] op_sel_hi:[1,0,1]\n"
    "v_cvt_pk_bf16_f32 v252, v232, v236\n"
    "v_cvt_pk_bf16_f32 v254, v234, v238\n"
    "v_cvt_pk_bf16_f32 v253, v233, v237\n"
    "v_cvt_pk_bf16_f32 v255, v235, v239\n"
    "v_mov_b32 v26, 0\n"
    "v_permlane32_swap_b32 v252, v254\n"
    "v_permlane32_swap_b32 v253, v255\n"
    "v_mov_b32 v27, 0\n"
    "s_nop 0\n"
    "v_permlane16_swap_b32 v252, v253\n"
    "v_permlane16_swap_b32 v254, v255\n"
    "s_setpc_b64 s[76:77]\n"
    ".Lf_PROCX1:\n"
    "buffer_store_dwordx4 v[252:255], v58, s[56:59], s80 offen sc1\n"
    "s_waitcnt lgkmcnt(0)\n"
    "buffer_store_dwordx4 v[248:251], v30, s[56:59], s80 offen sc1\n"
    "s_add_u32 s80, s80, 0x20000\n"
    "s_waitcnt lgkmcnt(0)\n"
    "s_barrier\n"
    "buffer_load_dwordx4 v[224:227], v19, s[12:15], s60 offen offset:0 nt\n"
    "buffer_load_dwordx4 v[228:231], v19, s[12:15], s60 offen offset:256 nt\n"
    "buffer_load_dwordx4 v[232:235], v60, s[16:19], s60 offen offset:0 nt\n"
    "buffer_load_dwordx4 v[236:239], v60, s[16:19], s60 offen offset:512 nt\n"
    "buffer_load_dwordx2 v[48:49], v20, s[20:23], s61 offen offset:0\n"
    "buffer_load_dwordx2 v[50:51], v20, s[20:23], s61 offen offset:128\n"
    "buffer_load_dwordx2 v[52:53], v20, s[24:27], s61 offen offset:0\n"
    "buffer_load_dwordx2 v[54:55], v20, s[24:27], s61 offen offset:128\n"
    "buffer_load_short_d16_hi v26, v21, s[32:35], s62 offen\n"
    "buffer_load_short_d16_hi v27, v21, s[28:31], s62 offen\n"
    "buffer_load_dword v56, v61, s[36:39], s62 offen\n"
    "buffer_load_dword v57, v61, s[40:43], s62 offen\n"
    "s_add_u32 s60, s60, 0x40000\n"
    "s_add_u32 s61, s61, 0x20000\n"
    "s_add_u32 s62, s62, 0x400\n"
    "s_waitcnt vmcnt(16)\n"
    ".p2align 3\n"
    "v_perm_b32 v176, v68, v64, s78\n"
    "v_perm_b32 v177, v68, v64, s79\n"
    "v_perm_b32 v178, v69, v65, s78\n"
    "v_perm_b32 v179, v69, v65, s79\n"
    "v_perm_b32 v180, v70, v66, s78\n"
    "v_perm_b32 v181, v70, v66, s79\n"
    "v_perm_b32 v182, v71, v67, s78\n"
    "v_perm_b32 v183, v71, v67, s79\n"
    "v_perm_b32 v160, v80, v72, s78\n"
    "v_perm_b32 v168, v76, v84, s78\n"
    "v_perm_b32 v161, v80, v72, s79\n"
    "v_perm_b32 v169, v76, v84, s79\n"
    "v_perm_b32 v162, v81, v73, s78\n"
    "v_perm_b32 v170, v77, v85, s78\n"
    "v_perm_b32 v163, v81, v73, s79\n"
    "v_perm_b32 v171, v77, v85, s79\n"
    "v_perm_b32 v164, v82, v74, s78\n"
    "v_perm_b32 v172, v78, v86, s78\n"
    "v_perm_b32 v165, v82, v74, s79\n"
    "v_perm_b32 v173, v78, v86, s79\n"
    "v_perm_b32 v166, v83, v75, s78\n"
    "v_perm_b32 v174, v79, v87, s78\n"
    "v_perm_b32 v167, v83, v75, s79\n"
    "v_perm_b32 v175, v79, v87, s79\n"
    "v_dot2_f32_bf16 v72, v176, v168, 0\n"
    "v_dot2_f32_bf16 v73, v177, v169, 0\n"
    "v_dot2_f32_bf16 v74, v178, v170, 0\n"
    "v_dot2_f32_bf16 v75, v179, v171, 0\n"
    "v_dot2_f32_bf16 v76, v180, v172, 0\n"
    "v_dot2_f32_bf16 v77, v181, v173, 0\n"
    "v_dot2_f32_bf16 v78, v182, v174, 0\n"
    "v_dot2_f32_bf16 v79, v183, v175, 0\n"
    "v_dot2_f32_bf16 v64, v176, v160, 0 neg_hi:[0,1,0]\n"
    "v_dot2_f32_bf16 v65, v177, v161, 0 neg_hi:[0,1,0]\n"
    "v_dot2_f32_bf16 v66, v178, v162, 0 neg_hi:[0,1,0]\n"
    "v_dot2_f32_bf16 v67, v179, v163, 0 neg_hi:[0,1,0]\n"
    "v_dot2_f32_bf16 v68, v180, v164, 0 neg_hi:[0,1,0]\n"
    "v_dot2_f32_bf16 v69, v181, v165, 0 neg_hi:[0,1,0]\n"
    "v_dot2_f32_bf16 v70, v182, v166, 0 neg_hi:[0,1,0]\n"
    "v_dot2_f32_bf16 v71, v183, v167, 0 neg_hi:[0,1,0]\n"
    "v_cvt_pk_bf16_f32 v82, v72, v73\n"
    "v_cvt_pk_bf16_f32 v83, v74, v75\n"
    "v_cvt_pk_bf16_f32 v86, v76, v77\n"
    "v_cvt_pk_bf16_f32 v87, v78, v79\n"
    "v_cvt_pk_bf16_f32 v80, v64, v65\n"
    "v_cvt_pk_bf16_f32 v81, v66, v67\n"
    "v_cvt_pk_bf16_f32 v84, v68, v69\n"
    "v_cvt_pk_bf16_f32 v85, v70, v71\n"
    "ds_write_b128 v18, v[80:83] offset:0\n"
    "ds_write_b128 v18, v[84:87] offset:2176\n"
    "s_waitcnt vmcnt(12)\n"
    "v_readfirstlane_b32 s74, v8\n"
    "v_readfirstlane_b32 s75, v9\n"
    "s_add_u32 s74, s74, 0x7f4a7c15\n"
    "s_addc_u32 s75, s75, 0x9e3779b9\n"
    "v_mov_b32 v240, 126976\n"
    "v_mov_b32 v241, 1\n"
    "s_mov_b64 exec, 1\n"
    "ds_add_rtn_u32 v240, v240, v241\n"
    "s_mov_b64 exec, -1\n"
    "s_waitcnt lgkmcnt(0)\n"
    "v_readfirstlane_b32 s88, v240\n"
    "s_cmp_eq_u32 s88, 3\n"
    "s_cbranch_scc0 .Lf_FBACK0\n"
    "v_mov_b32 v242, s74\n"
    "v_mov_b32 v243, s75\n"
    "v_mul_u32_u24 v241, 0x20000, v25\n"
    "s_add_u32 s88, s81, 0x0\n"
    "s_mov_b64 exec, 0x1\n"
    "buffer_store_dwordx2 v[242:243], v241, s[56:59], s88 offen sc1\n"
    "s_mov_b64 exec, -1\n"
    ".Lf_FBACK0:\n"
    "buffer_load_dwordx4 v[88:91], v3, s[44:47], s82 offen offset:2048 nt\n"
    "buffer_load_dwordx4 v[92:95], v3, s[44:47], s82 offen offset:2176 nt\n"
    "buffer_load_dwordx4 v[96:99], v3, s[48:51], s84 offen offset:2048\n"
    "buffer_load_dwordx4 v[100:103], v3, s[48:51], s84 offen offset:2176\n"
    "buffer_load_dwordx4 v[104:107], v3, s[52:55], s84 offen offset:2048\n"
    "buffer_load_dwordx4 v[108:111], v3, s[52:55], s84 offen offset:2176\n"
    "s_waitcnt vmcnt(6)\n"
    "s_call_b64 s[76:77], .Lf_PROC1\n"
    "buffer_store_dwordx4 v[252:255], v58, s[56:59], s80 offen sc1\n"
    "s_waitcnt lgkmcnt(0)\n"
    "buffer_store_dwordx4 v[248:251], v30, s[56:59], s80 offen sc1\n"
    "s_add_u32 s80, s80, 0x20000\n"
    "buffer_load_dwordx4 v[224:227], v19, s[12:15], s60 offen offset:0 nt\n"
    "buffer_load_dwordx4 v[228:231], v19, s[12:15], s60 offen offset:256 nt\n"
    "buffer_load_dwordx4 v[232:235], v60, s[16:19], s60 offen offset:0 nt\n"
    "buffer_load_dwordx4 v[236:239], v60, s[16:19], s60 offen offset:512 nt\n"
    "buffer_load_dwordx2 v[48:49], v20, s[20:23], s61 offen offset:0\n"
    "buffer_load_dwordx2 v[50:51], v20, s[20:23], s61 offen offset:128\n"
    "buffer_load_dwordx2 v[52:53], v20, s[24:27], s61 offen offset:0\n"
    "buffer_load_dwordx2 v[54:55], v20, s[24:27], s61 offen offset:128\n"
    "buffer_load_short_d16_hi v26, v21, s[32:35], s62 offen\n"
    "buffer_load_short_d16_hi v27, v21, s[28:31], s62 offen\n"
    "buffer_load_dword v56, v61, s[36:39], s62 offen\n"
    "buffer_load_dword v57, v61, s[40:43], s62 offen\n"
    "s_add_u32 s60, s60, 0x40000\n"
    "s_add_u32 s61, s61, 0x20000\n"
    "s_add_u32 s62, s62, 0x400\n"
    "s_waitcnt vmcnt(14)\n"
    ".p2align 3\n"
    "v_perm_b32 v176, v92, v88, s78\n"
    "v_perm_b32 v177, v92, v88, s79\n"
    "v_perm_b32 v178, v93, v89, s78\n"
    "v_perm_b32 v179, v93, v89, s79\n"
    "v_perm_b32 v180, v94, v90, s78\n"
    "v_perm_b32 v181, v94, v90, s79\n"
    "v_perm_b32 v182, v95, v91, s78\n"
    "v_perm_b32 v183, v95, v91, s79\n"
    "v_perm_b32 v160, v104, v96, s78\n"
    "v_perm_b32 v168, v100, v108, s78\n"
    "v_perm_b32 v161, v104, v96, s79\n"
    "v_perm_b32 v169, v100, v108, s79\n"
    "v_perm_b32 v162, v105, v97, s78\n"
    "v_perm_b32 v170, v101, v109, s78\n"
    "v_perm_b32 v163, v105, v97, s79\n"
    "v_perm_b32 v171, v101, v109, s79\n"
    "v_perm_b32 v164, v106, v98, s78\n"
    "v_perm_b32 v172, v102, v110, s78\n"
    "v_perm_b32 v165, v106, v98, s79\n"
    "v_perm_b32 v173, v102, v110, s79\n"
    "v_perm_b32 v166, v107, v99, s78\n"
    "v_perm_b32 v174, v103, v111, s78\n"
    "v_perm_b32 v167, v107, v99, s79\n"
    "v_perm_b32 v175, v103, v111, s79\n"
    "v_dot2_f32_bf16 v96, v176, v168, 0\n"
    "v_dot2_f32_bf16 v97, v177, v169, 0\n"
    "v_dot2_f32_bf16 v98, v178, v170, 0\n"
    "v_dot2_f32_bf16 v99, v179, v171, 0\n"
    "v_dot2_f32_bf16 v100, v180, v172, 0\n"
    "v_dot2_f32_bf16 v101, v181, v173, 0\n"
    "v_dot2_f32_bf16 v102, v182, v174, 0\n"
    "v_dot2_f32_bf16 v103, v183, v175, 0\n"
    "v_dot2_f32_bf16 v88, v176, v160, 0 neg_hi:[0,1,0]\n"
    "v_dot2_f32_bf16 v89, v177, v161, 0 neg_hi:[0,1,0]\n"
    "v_dot2_f32_bf16 v90, v178, v162, 0 neg_hi:[0,1,0]\n"
    "v_dot2_f32_bf16 v91, v179, v163, 0 neg_hi:[0,1,0]\n"
    "v_dot2_f32_bf16 v92, v180, v164, 0 neg_hi:[0,1,0]\n"
    "v_dot2_f32_bf16 v93, v181, v165, 0 neg_hi:[0,1,0]\n"
    "v_dot2_f32_bf16 v94, v182, v166, 0 neg_hi:[0,1,0]\n"
    "v_dot2_f32_bf16 v95, v183, v167, 0 neg_hi:[0,1,0]\n"
    "v_cvt_pk_bf16_f32 v106, v96, v97\n"
    "v_cvt_pk_bf16_f32 v107, v98, v99\n"
    "v_cvt_pk_bf16_f32 v110, v100, v101\n"
    "v_cvt_pk_bf16_f32 v111, v102, v103\n"
    "v_cvt_pk_bf16_f32 v104, v88, v89\n"
    "v_cvt_pk_bf16_f32 v105, v90, v91\n"
    "v_cvt_pk_bf16_f32 v108, v92, v93\n"
    "v_cvt_pk_bf16_f32 v109, v94, v95\n"
    "ds_write_b128 v18, v[104:107] offset:128\n"
    "ds_write_b128 v18, v[108:111] offset:2304\n"
    "s_waitcnt lgkmcnt(0)\n"
    "s_barrier\n"
    "ds_read_b128 a[128:131], v10 offset:0\n"
    "ds_read_b128 a[132:135], v10 offset:1088\n"
    "ds_read_b128 a[136:139], v10 offset:2176\n"
    "ds_read_b128 a[140:143], v10 offset:3264\n"
    "ds_read_b128 a[144:147], v11 offset:0\n"
    "ds_read_b128 a[148:151], v11 offset:1088\n"
    "ds_read_b128 a[152:155], v11 offset:2176\n"
    "ds_read_b128 a[156:159], v11 offset:3264\n"
    "ds_read_b128 a[160:163], v12 offset:0\n"
    "ds_read_b128 a[164:167], v12 offset:1088\n"
    "ds_read_b128 a[168:171], v12 offset:2176\n"
    "ds_read_b128 a[172:175], v12 offset:3264\n"
    "ds_read_b128 a[176:179], v13 offset:0\n"
    "ds_read_b128 a[180:183], v13 offset:1088\n"
    "ds_read_b128 a[184:187], v13 offset:2176\n"
    "ds_read_b128 a[188:191], v13 offset:3264\n"
    "s_mov_b64 exec, 0xffff\n"
    "buffer_load_dwordx2 v[22:23], v24, s[56:59], 0 offen sc1\n"
    "s_mov_b64 exec, -1\n"
    "s_waitcnt vmcnt(35)\n"
    "v_and_b32 v112, 0x7fff7fff, v112\n"
    "v_and_b32 v113, 0x7fff7fff, v113\n"
    "v_and_b32 v114, 0x7fff7fff, v114\n"
    "v_and_b32 v115, 0x7fff7fff, v115\n"
    "v_pk_max_u16 v112, v112, s83\n"
    "v_pk_max_u16 v113, v113, s83\n"
    "v_pk_max_u16 v114, v114, s83\n"
    "v_pk_max_u16 v115, v115, s83\n"
    "v_pk_min_u16 v112, v112, s93\n"
    "v_pk_min_u16 v113, v113, s93\n"
    "v_pk_min_u16 v114, v114, s93\n"
    "v_pk_min_u16 v115, v115, s93\n"
    "v_lshlrev_b32 v140, 16, v112\n"
    "v_and_b32 v141, 0xffff0000, v112\n"
    "v_lshlrev_b32 v142, 16, v113\n"
    "v_and_b32 v143, 0xffff0000, v113\n"
    "v_lshlrev_b32 v144, 16, v114\n"
    "v_and_b32 v145, 0xffff0000, v114\n"
    "v_lshlrev_b32 v146, 16, v115\n"
    "v_and_b32 v147, 0xffff0000, v115\n"
    "v_log_f32 v128, v140\n"
    "v_log_f32 v129, v141\n"
    "v_log_f32 v130, v142\n"
    "v_log_f32 v131, v143\n"
    "v_log_f32 v132, v144\n"
    "v_log_f32 v133, v145\n"
    "v_log_f32 v134, v146\n"
    "v_log_f32 v135, v147\n"
    "v_rcp_f32 v140, v140\n"
    "v_rcp_f32 v141, v141\n"
    "v_rcp_f32 v142, v142\n"
    "v_rcp_f32 v143, v143\n"
    "v_rcp_f32 v144, v144\n"
    "v_rcp_f32 v145, v145\n"
    "v_rcp_f32 v146, v146\n"
    "v_rcp_f32 v147, v147\n"
    "v_add_f32 v128, 0xc1f00000, v128\n"
    "v_add_f32 v129, 0xc1f00000, v129\n"
    "v_add_f32 v130, 0xc1f00000, v130\n"
    "v_add_f32 v131, 0xc1f00000, v131\n"
    "v_add_f32 v132, 0xc1f00000, v132\n"
    "v_add_f32 v133, 0xc1f00000, v133\n"
    "v_add_f32 v134, 0xc1f00000, v134\n"
    "v_add_f32 v135, 0xc1f00000, v135\n"
    "v_cvt_pk_bf16_f32 v136, v140, v141\n"
    "v_cvt_pk_bf16_f32 v137, v142, v143\n"
    "v_cvt_pk_bf16_f32 v138, v144, v145\n"
    "v_cvt_pk_bf16_f32 v139, v146, v147\n"
    "ds_write_b128 v117, v[128:131] offset:0\n"
    "ds_write_b128 v117, v[132:135] offset:16\n"
    "ds_write_b128 v117, v[136:139] offset:32\n"
    "v_lshrrev_b32 v9, 4, v25\n"
    "v_mad_u32_u24 v9, v9, 48, s64\n"
    "ds_read_b128 v[32:35], v9 offset:0\n"
    "ds_read_b128 v[36:39], v9 offset:16\n"
    "ds_read_b128 v[40:43], v9 offset:192\n"
    "ds_read_b128 v[44:47], v9 offset:208\n"
    "v_readfirstlane_b32 s44, v24\n"
    "s_mov_b64 vcc, 0\n"
    "s_waitcnt vmcnt(0)\n"
    "s_call_b64 s[76:77], .Lf_SPINC\n"
    "s_branch .Lf_SPINY\n"
    ".Lf_SPIN:\n"
    "s_mov_b64 exec, 0xffff\n"
    "buffer_load_dwordx2 v[22:23], v24, s[56:59], 0 offen sc1\n"
    "s_mov_b64 exec, -1\n"
    "s_waitcnt vmcnt(0)\n"
    ".Lf_SPINC:\n"
    "v_cmp_ne_u64 s[88:89], s[74:75], v[22:23]\n"
    "s_and_b32 s88, s88, 0xffff\n"
    "s_cbranch_scc0 .Lf_SPINX\n"
    "s_sub_u32 s90, s90, 1\n"
    "s_cbranch_scc1 .Lf_SPINT\n"
    "s_sleep 1\n"
    "s_branch .Lf_SPIN\n"
    ".Lf_SPINT:\n"
    "s_mov_b32 s90, 0\n"
    ".Lf_SPINX:\n"
    "s_setpc_b64 s[76:77]\n"
    ".Lf_SPINY:\n"
    "s_waitcnt vmcnt(13)\n"
    "v_mov_b32 v240, 126980\n"
    "v_mov_b32 v241, 1\n"
    "s_mov_b64 exec, 1\n"
    "ds_add_rtn_u32 v240, v240, v241\n"
    "s_mov_b64 exec, -1\n"
    "buffer_load_dwordx4 a[192:195], v0, s[56:59], s72 offen offset:0 sc1\n"
    "buffer_load_dwordx4 a[196:199], v0, s[56:59], s72 offen offset:1024 sc1\n"
    "buffer_load_dwordx4 a[200:203], v0, s[56:59], s72 offen offset:2048 sc1\n"
    "buffer_load_dwordx4 a[204:207], v0, s[56:59], s72 offen offset:3072 sc1\n"
    "buffer_load_dwordx4 a[208:211], v1, s[56:59], s72 offen offset:0 sc1\n"
    "buffer_load_dwordx4 a[212:215], v1, s[56:59], s72 offen offset:1024 sc1\n"
    "buffer_load_dwordx4 a[216:219], v1, s[56:59], s72 offen offset:2048 sc1\n"
    "buffer_load_dwordx4 a[220:223], v1, s[56:59], s72 offen offset:3072 sc1\n"
    "s_add_u32 s72, s72, 0x8000\n"
    "buffer_load_dwordx4 a[224:227], v0, s[56:59], s72 offen offset:0 sc1\n"
    "buffer_load_dwordx4 a[228:231], v0, s[56:59], s72 offen offset:1024 sc1\n"
    "buffer_load_dwordx4 a[232:235], v0, s[56:59], s72 offen offset:2048 sc1\n"
    "buffer_load_dwordx4 a[236:239], v0, s[56:59], s72 offen offset:3072 sc1\n"
    "buffer_load_dwordx4 a[240:243], v1, s[56:59], s72 offen offset:0 sc1\n"
    "buffer_load_dwordx4 a[244:247], v1, s[56:59], s72 offen offset:1024 sc1\n"
    "buffer_load_dwordx4 a[248:251], v1, s[56:59], s72 offen offset:2048 sc1\n"
    "buffer_load_dwordx4 a[252:255], v1, s[56:59], s72 offen offset:3072 sc1\n"
    "s_add_u32 s72, s72, 0x8000\n"
    "buffer_load_dwordx4 v[192:195], v0, s[56:59], s72 offen offset:0 sc1\n"
    "buffer_load_dwordx4 v[196:199], v0, s[56:59], s72 offen offset:1024 sc1\n"
    "buffer_load_dwordx4 v[200:203], v0, s[56:59], s72 offen offset:2048 sc1\n"
    "buffer_load_dwordx4 v[204:207], v0, s[56:59], s72 offen offset:3072 sc1\n"
    "buffer_load_dwordx4 v[208:211], v1, s[56:59], s72 offen offset:0 sc1\n"
    "buffer_load_dwordx4 v[212:215], v1, s[56:59], s72 offen offset:1024 sc1\n"
    "buffer_load_dwordx4 v[216:219], v1, s[56:59], s72 offen offset:2048 sc1\n"
    "buffer_load_dwordx4 v[220:223], v1, s[56:59], s72 offen offset:3072 sc1\n"
    "s_waitcnt lgkmcnt(0)\n"
    "v_readfirstlane_b32 s88, v240\n"
    "s_cmp_eq_u32 s88, 3\n"
    "s_cbranch_scc0 .Lf_FBACK1\n"
    "v_mov_b32 v242, s74\n"
    "v_mov_b32 v243, s75\n"
    "v_mul_u32_u24 v241, 0x20000, v25\n"
    "s_add_u32 s88, s81, 0x8000\n"
    "s_mov_b64 exec, 0x1\n"
    "buffer_store_dwordx2 v[242:243], v241, s[56:59], s88 offen sc1\n"
    "s_mov_b64 exec, -1\n"
    ".Lf_FBACK1:\n"
    "s_waitcnt vmcnt(25)\n"
    "s_call_b64 s[76:77], .Lf_PROC1\n"
    "buffer_store_dwordx4 v[252:255], v58, s[56:59], s80 offen sc1\n"
    "s_waitcnt lgkmcnt(0)\n"
    "buffer_store_dwordx4 v[248:251], v30, s[56:59], s80 offen sc1\n"
    "s_add_u32 s80, s80, 0x20000\n"
    "buffer_load_dwordx4 v[224:227], v19, s[12:15], s60 offen offset:0 nt\n"
    "buffer_load_dwordx4 v[228:231], v19, s[12:15], s60 offen offset:256 nt\n"
    "buffer_load_dwordx4 v[232:235], v60, s[16:19], s60 offen offset:0 nt\n"
    "buffer_load_dwordx4 v[236:239], v60, s[16:19], s60 offen offset:512 nt\n"
    "buffer_load_dwordx2 v[48:49], v20, s[20:23], s61 offen offset:0\n"
    "buffer_load_dwordx2 v[50:51], v20, s[20:23], s61 offen offset:128\n"
    "buffer_load_dwordx2 v[52:53], v20, s[24:27], s61 offen offset:0\n"
    "buffer_load_dwordx2 v[54:55], v20, s[24:27], s61 offen offset:128\n"
    "buffer_load_short_d16_hi v26, v21, s[32:35], s62 offen\n"
    "buffer_load_short_d16_hi v27, v21, s[28:31], s62 offen\n"
    "buffer_load_dword v56, v61, s[36:39], s62 offen\n"
    "buffer_load_dword v57, v61, s[40:43], s62 offen\n"
    "s_waitcnt lgkmcnt(0)\n"
    ".Lf_AGAIN:\n"
    "s_waitcnt vmcnt(34)\n"
    "v_mfma_f32_16x16x32_bf16 v[64:67], a[192:195], a[128:131], v[32:35]\n"
    "v_mfma_f32_16x16x32_bf16 v[64:67], a[196:199], a[132:135], v[64:67]\n"
    "v_mfma_f32_16x16x32_bf16 v[64:67], a[200:203], a[136:139], v[64:67]\n"
    "v_add_u32 v19, s63, v0\n"
    "v_mfma_f32_16x16x32_bf16 v[64:67], a[204:207], a[140:143], v[64:67]\n"
    "v_add_u32 v20, s66, v0\n"
    "s_waitcnt vmcnt(30)\n"
    "v_mfma_f32_16x16x32_bf16 v[80:83], a[208:211], a[128:131], v[36:39]\n"
    "v_add_u32 v21, s67, v0\n"
    "v_mfma_f32_16x16x32_bf16 v[80:83], a[212:215], a[132:135], v[80:83]\n"
    "v_mfma_f32_16x16x32_bf16 v[80:83], a[216:219], a[136:139], v[80:83]\n"
    "v_mfma_f32_16x16x32_bf16 v[80:83], a[220:223], a[140:143], v[80:83]\n"
    "v_mfma_f32_16x16x32_bf16 v[68:71], a[192:195], a[144:147], v[32:35]\n"
    "v_mfma_f32_16x16x32_bf16 v[68:71], a[196:199], a[148:151], v[68:71]\n"
    "v_mfma_f32_16x16x32_bf16 v[68:71], a[200:203], a[152:155], v[68:71]\n"
    "v_mfma_f32_16x16x32_bf16 v[68:71], a[204:207], a[156:159], v[68:71]\n"
    "v_mfma_f32_16x16x32_bf16 v[84:87], a[208:211], a[144:147], v[36:39]\n"
    "v_exp_f32 v2, v64\n"
    "v_mfma_f32_16x16x32_bf16 v[84:87], a[212:215], a[148:151], v[84:87]\n"
    "v_exp_f32 v3, v65\n"
    "v_mfma_f32_16x16x32_bf16 v[84:87], a[216:219], a[152:155], v[84:87]\n"
    "v_exp_f32 v4, v66\n"
    "v_mfma_f32_16x16x32_bf16 v[84:87], a[220:223], a[156:159], v[84:87]\n"
    "v_exp_f32 v5, v67\n"
    "v_mfma_f32_16x16x32_bf16 v[72:75], a[192:195], a[160:163], v[32:35]\n"
    "v_cvt_pk_bf16_f32 v128, v2, v3\n"
    "v_cvt_pk_bf16_f32 v129, v4, v5\n"
    "v_mfma_f32_16x16x32_bf16 v[72:75], a[196:199], a[164:167], v[72:75]\n"
    "v_exp_f32 v6, v80\n"
    "v_mfma_f32_16x16x32_bf16 v[72:75], a[200:203], a[168:171], v[72:75]\n"
    "v_exp_f32 v7, v81\n"
    "v_mfma_f32_16x16x32_bf16 v[72:75], a[204:207], a[172:175], v[72:75]\n"
    "v_exp_f32 v2, v82\n"
    "v_mfma_f32_16x16x32_bf16 v[88:91], a[208:211], a[160:163], v[36:39]\n"
    "v_cvt_pk_bf16_f32 v130, v6, v7\n"
    "v_mfma_f32_16x16x32_bf16 v[88:91], a[212:215], a[164:167], v[88:91]\n"
    "v_exp_f32 v3, v83\n"
    "v_mfma_f32_16x16x32_bf16 v[88:91], a[216:219], a[168:171], v[88:91]\n"
    "v_exp_f32 v4, v68\n"
    "v_mfma_f32_16x16x32_bf16 v[88:91], a[220:223], a[172:175], v[88:91]\n"
    "v_exp_f32 v5, v69\n"
    "v_mfma_f32_16x16x32_bf16 v[76:79], a[192:195], a[176:179], v[32:35]\n"
    "v_cvt_pk_bf16_f32 v131, v2, v3\n"
    "v_mfma_f32_16x16x32_bf16 v[76:79], a[196:199], a[180:183], v[76:79]\n"
    "v_exp_f32 v6, v70\n"
    "v_mfma_f32_16x16x32_bf16 v[76:79], a[200:203], a[184:187], v[76:79]\n"
    "v_exp_f32 v7, v71\n"
    "v_mfma_f32_16x16x32_bf16 v[76:79], a[204:207], a[188:191], v[76:79]\n"
    "v_cvt_pk_bf16_f32 v132, v4, v5\n"
    "v_cvt_pk_bf16_f32 v133, v6, v7\n"
    "v_mfma_f32_16x16x32_bf16 v[92:95], a[208:211], a[176:179], v[36:39]\n"
    "v_mfma_f32_16x16x32_bf16 v[92:95], a[212:215], a[180:183], v[92:95]\n"
    "v_exp_f32 v2, v84\n"
    "v_mfma_f32_16x16x32_bf16 v[92:95], a[216:219], a[184:187], v[92:95]\n"
    "v_exp_f32 v3, v85\n"
    "v_mfma_f32_16x16x32_bf16 v[92:95], a[220:223], a[188:191], v[92:95]\n"
    "v_exp_f32 v4, v86\n"
    "s_waitcnt vmcnt(26)\n"
    "v_mfma_f32_16x16x32_bf16 v[96:99], a[224:227], a[128:131], v[40:43]\n"
    "v_exp_f32 v5, v87\n"
    "v_mfma_f32_16x16x32_bf16 v[96:99], a[228:231], a[132:135], v[96:99]\n"
    "v_cvt_pk_bf16_f32 v134, v2, v3\n"
    "v_cvt_pk_bf16_f32 v135, v4, v5\n"
    "v_mfma_f32_16x16x32_bf16 v[96:99], a[232:235], a[136:139], v[96:99]\n"
    "v_exp_f32 v6, v72\n"
    "v_mfma_f32_16x16x32_bf16 v[96:99], a[236:239], a[140:143], v[96:99]\n"
    "buffer_load_dwordx4 a[12:15], v0, s[8:11], s72 offen offset:0 sc1\n"
    "v_mfma_f32_16x16x32_bf16 v[100:103], a[224:227], a[144:147], v[40:43]\n"
    "v_exp_f32 v7, v73\n"
    "v_mfma_f32_16x16x32_bf16 v[100:103], a[228:231], a[148:151], v[100:103]\n"
    "ds_read_b128 v[32:35], v9 offset:384\n"
    "v_cvt_pk_bf16_f32 v136, v6, v7\n"
    "v_mfma_f32_16x16x32_bf16 v[100:103], a[232:235], a[152:155], v[100:103]\n"
    "v_exp_f32 v2, v74\n"
    "v_mfma_f32_16x16x32_bf16 v[100:103], a[236:239], a[156:159], v[100:103]\n"
    "buffer_load_dwordx4 a[28:31], v0, s[8:11], s72 offen offset:1024 sc1\n"
    "v_mfma_f32_16x16x32_bf16 v[104:107], a[224:227], a[160:163], v[40:43]\n"
    "v_exp_f32 v3, v75\n"
    "v_mfma_f32_16x16x32_bf16 v[104:107], a[228:231], a[164:167], v[104:107]\n"
    "ds_read_b128 v[36:39], v9 offset:400\n"
    "v_cvt_pk_bf16_f32 v137, v2, v3\n"
    "v_mfma_f32_16x16x32_bf16 v[104:107], a[232:235], a[168:171], v[104:107]\n"
    "v_exp_f32 v4, v88\n"
    "v_mfma_f32_16x16x32_bf16 v[104:107], a[236:239], a[172:175], v[104:107]\n"
    "buffer_load_dwordx4 a[44:47], v0, s[8:11], s72 offen offset:2048 sc1\n"
    "v_mfma_f32_16x16x32_bf16 v[108:111], a[224:227], a[176:179], v[40:43]\n"
    "v_exp_f32 v5, v89\n"
    "v_mfma_f32_16x16x32_bf16 v[108:111], a[228:231], a[180:183], v[108:111]\n"
    "ds_read_b128 v[60:63], v9 offset:32\n"
    "v_cvt_pk_bf16_f32 v138, v4, v5\n"
    "v_mfma_f32_16x16x32_bf16 v[108:111], a[232:235], a[184:187], v[108:111]\n"
    "v_exp_f32 v6, v90\n"
    "v_mfma_f32_16x16x32_bf16 v[108:111], a[236:239], a[188:191], v[108:111]\n"
    "buffer_load_dwordx4 a[60:63], v0, s[8:11], s72 offen offset:3072 sc1\n"
    "s_waitcnt vmcnt(26)\n"
    "v_mfma_f32_16x16x32_bf16 v[112:115], a[240:243], a[128:131], v[44:47]\n"
    "v_exp_f32 v7, v91\n"
    "v_mfma_f32_16x16x32_bf16 v[112:115], a[244:247], a[132:135], v[112:115]\n"
    "v_exp_f32 v2, v76\n"
    "v_mfma_f32_16x16x32_bf16 v[112:115], a[248:251], a[136:139], v[112:115]\n"
    "v_exp_f32 v3, v77\n"
    "v_mfma_f32_16x16x32_bf16 v[112:115], a[252:255], a[140:143], v[112:115]\n"
    "buffer_load_dwordx4 a[76:79], v1, s[8:11], s72 offen offset:0 sc1\n"
    "v_cvt_pk_bf16_f32 v139, v6, v7\n"
    "v_mfma_f32_16x16x32_bf16 v[116:119], a[240:243], a[144:147], v[44:47]\n"
    "v_cvt_pk_bf16_f32 v140, v2, v3\n"
    "v_mfma_f32_16x16x32_bf16 v[116:119], a[244:247], a[148:151], v[116:119]\n"
    "v_exp_f32 v4, v78\n"
    "v_mfma_f32_16x16x32_bf16 v[116:119], a[248:251], a[152:155], v[116:119]\n"
    "v_exp_f32 v5, v79\n"
    "v_mfma_f32_16x16x32_bf16 v[116:119], a[252:255], a[156:159], v[116:119]\n"
    "buffer_load_dwordx4 a[92:95], v1, s[8:11], s72 offen offset:1024 sc1\n"
    "v_cvt_pk_bf16_f32 v141, v4, v5\n"
    "v_mfma_f32_16x16x32_bf16 v[120:123], a[240:243], a[160:163], v[44:47]\n"
    "v_mfma_f32_16x16x32_bf16 v[120:123], a[244:247], a[164:167], v[120:123]\n"
    "v_exp_f32 v6, v92\n"
    "v_mfma_f32_16x16x32_bf16 v[120:123], a[248:251], a[168:171], v[120:123]\n"
    "v_exp_f32 v7, v93\n"
    "v_mfma_f32_16x16x32_bf16 v[120:123], a[252:255], a[172:175], v[120:123]\n"
    "buffer_load_dwordx4 a[108:111], v1, s[8:11], s72 offen offset:2048 sc1\n"
    "v_cvt_pk_bf16_f32 v142, v6, v7\n"
    "v_mfma_f32_16x16x32_bf16 v[124:127], a[240:243], a[176:179], v[44:47]\n"
    "v_exp_f32 v2, v94\n"
    "v_mfma_f32_16x16x32_bf16 v[124:127], a[244:247], a[180:183], v[124:127]\n"
    "v_exp_f32 v3, v95\n"
    "v_mfma_f32_16x16x32_bf16 v[124:127], a[248:251], a[184:187], v[124:127]\n"
    "v_cvt_pk_bf16_f32 v143, v2, v3\n"
    "v_mfma_f32_16x16x32_bf16 v[124:127], a[252:255], a[188:191], v[124:127]\n"
    "buffer_load_dwordx4 a[124:127], v1, s[8:11], s72 offen offset:3072 sc1\n"
    "s_waitcnt lgkmcnt(0)\n"
    "s_cbranch_vccnz .Lf_STUB1\n"
    ".Lf_BACK1:\n"
    "v_mfma_f32_16x16x32_bf16 v[10:13], v[60:63], v[128:131], 0\n"
    "v_exp_f32 v2, v96\n"
    "v_mfma_f32_16x16x32_bf16 v[14:17], v[60:63], v[132:135], 0\n"
    "v_exp_f32 v3, v97\n"
    "v_mfma_f32_16x16x32_bf16 v[40:43], v[60:63], v[136:139], 0\n"
    "v_exp_f32 v4, v98\n"
    "v_mfma_f32_16x16x32_bf16 v[44:47], v[60:63], v[140:143], 0\n"
    "v_exp_f32 v5, v99\n"
    "s_waitcnt vmcnt(26)\n"
    "v_mfma_f32_16x16x32_bf16 v[64:67], v[192:195], a[128:131], v[32:35]\n"
    "v_cvt_pk_bf16_f32 v144, v2, v3\n"
    "v_cvt_pk_bf16_f32 v145, v4, v5\n"
    "v_mfma_f32_16x16x32_bf16 v[64:67], v[196:199], a[132:135], v[64:67]\n"
    "v_mfma_f32_16x16x32_bf16 v[64:67], v[200:203], a[136:139], v[64:67]\n"
    "v_exp_f32 v6, v112\n"
    "v_mfma_f32_16x16x32_bf16 v[64:67], v[204:207], a[140:143], v[64:67]\n"
    "s_add_u32 s72, s72, 0x8000\n"
    "buffer_load_dwordx4 a[192:195], v0, s[56:59], s72 offen offset:0 sc1\n"
    "v_mfma_f32_16x16x32_bf16 v[68:71], v[192:195], a[144:147], v[32:35]\n"
    "v_exp_f32 v7, v113\n"
    "v_mfma_f32_16x16x32_bf16 v[68:71], v[196:199], a[148:151], v[68:71]\n"
    "v_exp_f32 v2, v114\n"
    "v_mfma_f32_16x16x32_bf16 v[68:71], v[200:203], a[152:155], v[68:71]\n"
    "v_exp_f32 v3, v115\n"
    "v_mfma_f32_16x16x32_bf16 v[68:71], v[204:207], a[156:159], v[68:71]\n"
    "buffer_load_dwordx4 a[196:199], v0, s[56:59], s72 offen offset:1024 sc1\n"
    "v_mfma_f32_16x16x32_bf16 v[72:75], v[192:195], a[160:163], v[32:35]\n"
    "v_cvt_pk_bf16_f32 v146, v6, v7\n"
    "v_cvt_pk_bf16_f32 v147, v2, v3\n"
    "v_mfma_f32_16x16x32_bf16 v[72:75], v[196:199], a[164:167], v[72:75]\n"
    "v_exp_f32 v4, v100\n"
    "v_mfma_f32_16x16x32_bf16 v[72:75], v[200:203], a[168:171], v[72:75]\n"
    "v_mfma_f32_16x16x32_bf16 v[72:75], v[204:207], a[172:175], v[72:75]\n"
    "buffer_load_dwordx4 a[200:203], v0, s[56:59], s72 offen offset:2048 sc1\n"
    "v_mfma_f32_16x16x32_bf16 v[76:79], v[192:195], a[176:179], v[32:35]\n"
    "v_exp_f32 v5, v101\n"
    "v_mfma_f32_16x16x32_bf16 v[76:79], v[196:199], a[180:183], v[76:79]\n"
    "v_exp_f32 v6, v102\n"
    "v_mfma_f32_16x16x32_bf16 v[76:79], v[200:203], a[184:187], v[76:79]\n"
    "v_exp_f32 v7, v103\n"
    "v_mfma_f32_16x16x32_bf16 v[76:79], v[204:207], a[188:191], v[76:79]\n"
    "buffer_load_dwordx4 a[204:207], v0, s[56:59], s72 offen offset:3072 sc1\n"
    "v_cvt_pk_bf16_f32 v148, v4, v5\n"
    "s_waitcnt vmcnt(26)\n"
    "v_mfma_f32_16x16x32_bf16 v[80:83], v[208:211], a[128:131], v[36:39]\n"
    "v_exp_f32 v2, v116\n"
    "v_mfma_f32_16x16x32_bf16 v[80:83], v[212:215], a[132:135], v[80:83]\n"
    "v_exp_f32 v3, v117\n"
    "v_mfma_f32_16x16x32_bf16 v[80:83], v[216:219], a[136:139], v[80:83]\n"
    "v_cvt_pk_bf16_f32 v149, v6, v7\n"
    "v_cvt_pk_bf16_f32 v150, v2, v3\n"
    "v_mfma_f32_16x16x32_bf16 v[80:83], v[220:223], a[140:143], v[80:83]\n"
    "buffer_load_dwordx4 a[208:211], v1, s[56:59], s72 offen offset:0 sc1\n"
    "v_mfma_f32_16x16x32_bf16 v[84:87], v[208:211], a[144:147], v[36:39]\n"
    "v_exp_f32 v4, v118\n"
    "v_mfma_f32_16x16x32_bf16 v[84:87], v[212:215], a[148:151], v[84:87]\n"
    "v_exp_f32 v5, v119\n"
    "v_mfma_f32_16x16x32_bf16 v[84:87], v[216:219], a[152:155], v[84:87]\n"
    "v_cvt_pk_bf16_f32 v151, v4, v5\n"
    "v_mfma_f32_16x16x32_bf16 v[84:87], v[220:223], a[156:159], v[84:87]\n"
    "buffer_load_dwordx4 a[212:215], v1, s[56:59], s72 offen offset:1024 sc1\n"
    "v_mfma_f32_16x16x32_bf16 v[88:91], v[208:211], a[160:163], v[36:39]\n"
    "v_exp_f32 v6, v104\n"
    "v_mfma_f32_16x16x32_bf16 v[88:91], v[212:215], a[164:167], v[88:91]\n"
    "v_exp_f32 v7, v105\n"
    "v_mfma_f32_16x16x32_bf16 v[88:91], v[216:219], a[168:171], v[88:91]\n"
    "v_exp_f32 v2, v106\n"
    "v_mfma_f32_16x16x32_bf16 v[88:91], v[220:223], a[172:175], v[88:91]\n"
    "buffer_load_dwordx4 a[216:219], v1, s[56:59], s72 offen offset:2048 sc1\n"
    "v_cvt_pk_bf16_f32 v152, v6, v7\n"
    "v_mfma_f32_16x16x32_bf16 v[92:95], v[208:211], a[176:179], v[36:39]\n"
    "v_mfma_f32_16x16x32_bf16 v[92:95], v[212:215], a[180:183], v[92:95]\n"
    "v_exp_f32 v3, v107\n"
    "v_mfma_f32_16x16x32_bf16 v[92:95], v[216:219], a[184:187], v[92:95]\n"
    "v_exp_f32 v4, v120\n"
    "v_mfma_f32_16x16x32_bf16 v[92:95], v[220:223], a[188:191], v[92:95]\n"
    "buffer_load_dwordx4 a[220:223], v1, s[56:59], s72 offen offset:3072 sc1\n"
    "v_add_u32 v24, 0x8000, v24\n"
    "s_mov_b64 exec, 0xffff\n"
    "buffer_load_dwordx2 v[22:23], v24, s[56:59], 0 offen sc1\n"
    "s_mov_b64 exec, -1\n"
    "s_waitcnt vmcnt(13)\n"
    "v_mfma_f32_16x16x32_bf16 a[0:3], v[128:131], a[12:15], 0\n"
    "v_mfma_f32_16x16x32_bf16 a[4:7], v[132:135], a[12:15], 0\n"
    "v_cvt_pk_bf16_f32 v153, v2, v3\n"
    "v_mfma_f32_16x16x32_bf16 a[8:11], v[136:139], a[12:15], 0\n"
    "v_mfma_f32_16x16x32_bf16 v[160:163], v[140:143], a[12:15], 0\n"
    "buffer_load_dwordx4 v[192:195], v0, s[8:11], s72 offen offset:0 sc1\n"
    "v_mfma_f32_16x16x32_bf16 a[16:19], v[128:131], a[28:31], 0\n"
    "s_waitcnt vmcnt(30)\n"
    "v_mov_b32 v240, 126984\n"
    "v_mov_b32 v241, 1\n"
    "s_mov_b64 exec, 1\n"
    "ds_add_rtn_u32 v240, v240, v241\n"
    "s_mov_b64 exec, -1\n"
    "v_mfma_f32_16x16x32_bf16 a[20:23], v[132:135], a[28:31], 0\n"
    "ds_read_b128 v[32:35], v9 offset:576\n"
    "v_mfma_f32_16x16x32_bf16 a[24:27], v[136:139], a[28:31], 0\n"
    "v_mfma_f32_16x16x32_bf16 v[164:167], v[140:143], a[28:31], 0\n"
    "buffer_load_dwordx4 v[196:199], v0, s[8:11], s72 offen offset:1024 sc1\n"
    "v_mfma_f32_16x16x32_bf16 a[32:35], v[128:131], a[44:47], 0\n"
    "v_exp_f32 v5, v121\n"
    "v_mfma_f32_16x16x32_bf16 a[36:39], v[132:135], a[44:47], 0\n"
    "ds_read_b128 v[36:39], v9 offset:592\n"
    "v_cvt_pk_bf16_f32 v154, v4, v5\n"
    "v_mfma_f32_16x16x32_bf16 a[40:43], v[136:139], a[44:47], 0\n"
    "v_exp_f32 v6, v122\n"
    "v_mfma_f32_16x16x32_bf16 v[168:171], v[140:143], a[44:47], 0\n"
    "buffer_load_dwordx4 v[200:203], v0, s[8:11], s72 offen offset:2048 sc1\n"
    "v_mfma_f32_16x16x32_bf16 a[48:51], v[128:131], a[60:63], 0\n"
    "s_waitcnt lgkmcnt(0)\n"
    "v_readfirstlane_b32 s88, v240\n"
    "s_cmp_eq_u32 s88, 3\n"
    "s_cbranch_scc1 .Lf_FSTUB2\n"
    ".Lf_FBACK2:\n"
    "v_mfma_f32_16x16x32_bf16 a[52:55], v[132:135], a[60:63], 0\n"
    "ds_read_b128 v[60:63], v9 offset:224\n"
    "v_mfma_f32_16x16x32_bf16 a[56:59], v[136:139], a[60:63], 0\n"
    "v_mfma_f32_16x16x32_bf16 v[172:175], v[140:143], a[60:63], 0\n"
    "buffer_load_dwordx4 v[204:207], v0, s[8:11], s72 offen offset:3072 sc1\n"
    "s_waitcnt vmcnt(13)\n"
    "v_mfma_f32_16x16x32_bf16 a[64:67], v[128:131], a[76:79], 0\n"
    "v_exp_f32 v7, v123\n"
    "v_mfma_f32_16x16x32_bf16 a[68:71], v[132:135], a[76:79], 0\n"
    "v_exp_f32 v2, v108\n"
    "v_mfma_f32_16x16x32_bf16 a[72:75], v[136:139], a[76:79], 0\n"
    "v_exp_f32 v3, v109\n"
    "v_mfma_f32_16x16x32_bf16 v[176:179], v[140:143], a[76:79], 0\n"
    "buffer_load_dwordx4 v[208:211], v1, s[8:11], s72 offen offset:0 sc1\n"
    "v_cvt_pk_bf16_f32 v155, v6, v7\n"
    "v_mfma_f32_16x16x32_bf16 a[80:83], v[128:131], a[92:95], 0\n"
    "v_exp_f32 v4, v110\n"
    "v_mfma_f32_16x16x32_bf16 a[84:87], v[132:135], a[92:95], 0\n"
    "v_exp_f32 v5, v111\n"
    "v_mfma_f32_16x16x32_bf16 a[88:91], v[136:139], a[92:95], 0\n"
    "v_cvt_pk_bf16_f32 v156, v2, v3\n"
    "v_cvt_pk_bf16_f32 v157, v4, v5\n"
    "v_mfma_f32_16x16x32_bf16 v[180:183], v[140:143], a[92:95], 0\n"
    "buffer_load_dwordx4 v[212:215], v1, s[8:11], s72 offen offset:1024 sc1\n"
    "v_mfma_f32_16x16x32_bf16 a[96:99], v[128:131], a[108:111], 0\n"
    "v_exp_f32 v6, v124\n"
    "v_mfma_f32_16x16x32_bf16 a[100:103], v[132:135], a[108:111], 0\n"
    "v_exp_f32 v7, v125\n"
    "v_mfma_f32_16x16x32_bf16 a[104:107], v[136:139], a[108:111], 0\n"
    "v_cvt_pk_bf16_f32 v158, v6, v7\n"
    "v_mfma_f32_16x16x32_bf16 v[184:187], v[140:143], a[108:111], 0\n"
    "buffer_load_dwordx4 v[216:219], v1, s[8:11], s72 offen offset:2048 sc1\n"
    "v_mfma_f32_16x16x32_bf16 a[112:115], v[128:131], a[124:127], 0\n"
    "v_exp_f32 v2, v126\n"
    "v_mfma_f32_16x16x32_bf16 a[116:119], v[132:135], a[124:127], 0\n"
    "v_exp_f32 v3, v127\n"
    "v_mfma_f32_16x16x32_bf16 a[120:123], v[136:139], a[124:127], 0\n"
    "v_cvt_pk_bf16_f32 v159, v2, v3\n"
    "v_mfma_f32_16x16x32_bf16 v[188:191], v[140:143], a[124:127], 0\n"
    "buffer_load_dwordx4 v[220:223], v1, s[8:11], s72 offen offset:3072 sc1\n"
    "s_waitcnt lgkmcnt(0)\n"
    "s_cbranch_vccnz .Lf_STUB2\n"
    ".Lf_BACK2:\n"
    "v_mfma_f32_16x16x32_bf16 v[10:13], v[60:63], v[144:147], v[10:13]\n"
    "v_exp_f32 v2, v64\n"
    "v_mfma_f32_16x16x32_bf16 v[14:17], v[60:63], v[148:151], v[14:17]\n"
    "v_exp_f32 v3, v65\n"
    "v_mfma_f32_16x16x32_bf16 v[40:43], v[60:63], v[152:155], v[40:43]\n"
    "v_exp_f32 v4, v66\n"
    "v_mfma_f32_16x16x32_bf16 v[44:47], v[60:63], v[156:159], v[44:47]\n"
    "v_exp_f32 v5, v67\n"
    "s_waitcnt vmcnt(13)\n"
    "v_mfma_f32_16x16x32_bf16 v[96:99], a[192:195], a[128:131], v[32:35]\n"
    "v_cvt_pk_bf16_f32 v128, v2, v3\n"
    "v_mfma_f32_16x16x32_bf16 v[96:99], a[196:199], a[132:135], v[96:99]\n"
    "v_exp_f32 v6, v80\n"
    "v_mfma_f32_16x16x32_bf16 v[96:99], a[200:203], a[136:139], v[96:99]\n"
    "v_cvt_pk_bf16_f32 v129, v4, v5\n"
    "v_mfma_f32_16x16x32_bf16 v[96:99], a[204:207], a[140:143], v[96:99]\n"
    "s_waitcnt vmcnt(8)\n"
    "v_cmp_ne_u64 s[88:89], s[74:75], v[22:23]\n"
    "s_and_b32 s88, s88, 0xffff\n"
    "s_cbranch_scc1 .Lf_PSTUB1\n"
    ".Lf_PBACK1:\n"
    "s_add_u32 s72, s72, 0x8000\n"
    "buffer_load_dwordx4 a[224:227], v0, s[56:59], s72 offen offset:0 sc1\n"
    "v_mfma_f32_16x16x32_bf16 v[100:103], a[192:195], a[144:147], v[32:35]\n"
    "v_mfma_f32_16x16x32_bf16 v[100:103], a[196:199], a[148:151], v[100:103]\n"
    "v_mfma_f32_16x16x32_bf16 v[100:103], a[200:203], a[152:155], v[100:103]\n"
    "v_mfma_f32_16x16x32_bf16 v[100:103], a[204:207], a[156:159], v[100:103]\n"
    "buffer_load_dwordx4 a[228:231], v0, s[56:59], s72 offen offset:1024 sc1\n"
    "v_mfma_f32_16x16x32_bf16 v[104:107], a[192:195], a[160:163], v[32:35]\n"
    "v_exp_f32 v7, v81\n"
    "v_mfma_f32_16x16x32_bf16 v[104:107], a[196:199], a[164:167], v[104:107]\n"
    "v_exp_f32 v2, v82\n"
    "v_mfma_f32_16x16x32_bf16 v[104:107], a[200:203], a[168:171], v[104:107]\n"
    "v_exp_f32 v3, v83\n"
    "v_mfma_f32_16x16x32_bf16 v[104:107], a[204:207], a[172:175], v[104:107]\n"
    "buffer_load_dwordx4 a[232:235], v0, s[56:59], s72 offen offset:2048 sc1\n"
    "v_cvt_pk_bf16_f32 v130, v6, v7\n"
    "v_mfma_f32_16x16x32_bf16 v[108:111], a[192:195], a[176:179], v[32:35]\n"
    "v_cvt_pk_bf16_f32 v131, v2, v3\n"
    "v_mfma_f32_16x16x32_bf16 v[108:111], a[196:199], a[180:183], v[108:111]\n"
    "v_exp_f32 v4, v68\n"
    "v_mfma_f32_16x16x32_bf16 v[108:111], a[200:203], a[184:187], v[108:111]\n"
    "v_mfma_f32_16x16x32_bf16 v[108:111], a[204:207], a[188:191], v[108:111]\n"
    "buffer_load_dwordx4 a[236:239], v0, s[56:59], s72 offen offset:3072 sc1\n"
    "v_mfma_f32_16x16x32_bf16 v[112:115], a[208:211], a[128:131], v[36:39]\n"
    "v_exp_f32 v5, v69\n"
    "v_mfma_f32_16x16x32_bf16 v[112:115], a[212:215], a[132:135], v[112:115]\n"
    "v_exp_f32 v6, v70\n"
    "v_mfma_f32_16x16x32_bf16 v[112:115], a[216:219], a[136:139], v[112:115]\n"
    "v_exp_f32 v7, v71\n"
    "v_mfma_f32_16x16x32_bf16 v[112:115], a[220:223], a[140:143], v[112:115]\n"
    "buffer_load_dwordx4 a[240:243], v1, s[56:59], s72 offen offset:0 sc1\n"
    "v_cvt_pk_bf16_f32 v132, v4, v5\n"
    "v_mfma_f32_16x16x32_bf16 v[116:119], a[208:211], a[144:147], v[36:39]\n"
    "v_cvt_pk_bf16_f32 v133, v6, v7\n"
    "v_mfma_f32_16x16x32_bf16 v[116:119], a[212:215], a[148:151], v[116:119]\n"
    "v_exp_f32 v2, v84\n"
    "v_mfma_f32_16x16x32_bf16 v[116:119], a[216:219], a[152:155], v[116:119]\n"
    "v_mfma_f32_16x16x32_bf16 v[116:119], a[220:223], a[156:159], v[116:119]\n"
    "buffer_load_dwordx4 a[244:247], v1, s[56:59], s72 offen offset:1024 sc1\n"
    "v_mfma_f32_16x16x32_bf16 v[120:123], a[208:211], a[160:163], v[36:39]\n"
    "v_exp_f32 v3, v85\n"
    "v_mfma_f32_16x16x32_bf16 v[120:123], a[212:215], a[164:167], v[120:123]\n"
    "v_exp_f32 v4, v86\n"
    "v_mfma_f32_16x16x32_bf16 v[120:123], a[216:219], a[168:171], v[120:123]\n"
    "v_exp_f32 v5, v87\n"
    "v_mfma_f32_16x16x32_bf16 v[120:123], a[220:223], a[172:175], v[120:123]\n"
    "buffer_load_dwordx4 a[248:251], v1, s[56:59], s72 offen offset:2048 sc1\n"
    "v_cvt_pk_bf16_f32 v134, v2, v3\n"
    "v_mfma_f32_16x16x32_bf16 v[124:127], a[208:211], a[176:179], v[36:39]\n"
    "v_cvt_pk_bf16_f32 v135, v4, v5\n"
    "v_mfma_f32_16x16x32_bf16 v[124:127], a[212:215], a[180:183], v[124:127]\n"
    "v_exp_f32 v6, v72\n"
    "v_mfma_f32_16x16x32_bf16 v[124:127], a[216:219], a[184:187], v[124:127]\n"
    "v_exp_f32 v7, v73\n"
    "v_mfma_f32_16x16x32_bf16 v[124:127], a[220:223], a[188:191], v[124:127]\n"
    "buffer_load_dwordx4 a[252:255], v1, s[56:59], s72 offen offset:3072 sc1\n"
    "s_waitcnt vmcnt(12)\n"
    "v_mfma_f32_16x16x32_bf16 a[0:3], v[144:147], v[192:195], a[0:3]\n"
    "v_exp_f32 v2, v74\n"
    "v_mfma_f32_16x16x32_bf16 a[4:7], v[148:151], v[192:195], a[4:7]\n"
    "v_cvt_pk_bf16_f32 v136, v6, v7\n"
    "v_mfma_f32_16x16x32_bf16 a[8:11], v[152:155], v[192:195], a[8:11]\n"
    "v_exp_f32 v3, v75\n"
    "v_mfma_f32_16x16x32_bf16 v[160:163], v[156:159], v[192:195], v[160:163]\n"
    "buffer_load_dwordx4 a[12:15], v0, s[8:11], s72 offen offset:0 sc1\n"
    "v_mfma_f32_16x16x32_bf16 a[16:19], v[144:147], v[196:199], a[16:19]\n"
    "v_exp_f32 v4, v88\n"
    "v_mfma_f32_16x16x32_bf16 a[20:23], v[148:151], v[196:199], a[20:23]\n"
    "ds_read_b128 v[32:35], v9 offset:768\n"
    "v_mfma_f32_16x16x32_bf16 a[24:27], v[152:155], v[196:199], a[24:27]\n"
    "v_exp_f32 v5, v89\n"
    "v_mfma_f32_16x16x32_bf16 v[164:167], v[156:159], v[196:199], v[164:167]\n"
    "buffer_load_dwordx4 a[28:31], v0, s[8:11], s72 offen offset:1024 sc1\n"
    "v_mfma_f32_16x16x32_bf16 a[32:35], v[144:147], v[200:203], a[32:35]\n"
    "v_cvt_pk_bf16_f32 v137, v2, v3\n"
    "v_cvt_pk_bf16_f32 v138, v4, v5\n"
    "v_mfma_f32_16x16x32_bf16 a[36:39], v[148:151], v[200:203], a[36:39]\n"
    "ds_read_b128 v[36:39], v9 offset:784\n"
    "v_mfma_f32_16x16x32_bf16 a[40:43], v[152:155], v[200:203], a[40:43]\n"
    "v_exp_f32 v6, v90\n"
    "v_mfma_f32_16x16x32_bf16 v[168:171], v[156:159], v[200:203], v[168:171]\n"
    "buffer_load_dwordx4 a[44:47], v0, s[8:11], s72 offen offset:2048 sc1\n"
    "v_mfma_f32_16x16x32_bf16 a[48:51], v[144:147], v[204:207], a[48:51]\n"
    "v_exp_f32 v7, v91\n"
    "v_mfma_f32_16x16x32_bf16 a[52:55], v[148:151], v[204:207], a[52:55]\n"
    "ds_read_b128 v[60:63], v9 offset:416\n"
    "v_mfma_f32_16x16x32_bf16 a[56:59], v[152:155], v[204:207], a[56:59]\n"
    "v_exp_f32 v2, v76\n"
    "v_mfma_f32_16x16x32_bf16 v[172:175], v[156:159], v[204:207], v[172:175]\n"
    "buffer_load_dwordx4 a[60:63], v0, s[8:11], s72 offen offset:3072 sc1\n"
    "s_waitcnt vmcnt(12)\n"
    "v_mfma_f32_16x16x32_bf16 a[64:67], v[144:147], v[208:211], a[64:67]\n"
    "v_exp_f32 v3, v77\n"
    "v_mfma_f32_16x16x32_bf16 a[68:71], v[148:151], v[208:211], a[68:71]\n"
    "v_cvt_pk_bf16_f32 v139, v6, v7\n"
    "v_mfma_f32_16x16x32_bf16 a[72:75], v[152:155], v[208:211], a[72:75]\n"
    "v_exp_f32 v4, v78\n"
    "v_mfma_f32_16x16x32_bf16 v[176:179], v[156:159], v[208:211], v[176:179]\n"
    "buffer_load_dwordx4 a[76:79], v1, s[8:11], s72 offen offset:0 sc1\n"
    "v_mfma_f32_16x16x32_bf16 a[80:83], v[144:147], v[212:215], a[80:83]\n"
    "v_exp_f32 v5, v79\n"
    "v_mfma_f32_16x16x32_bf16 a[84:87], v[148:151], v[212:215], a[84:87]\n"
    "v_cvt_pk_bf16_f32 v140, v2, v3\n"
    "v_mfma_f32_16x16x32_bf16 a[88:91], v[152:155], v[212:215], a[88:91]\n"
    "v_exp_f32 v6, v92\n"
    "v_mfma_f32_16x16x32_bf16 v[180:183], v[156:159], v[212:215], v[180:183]\n"
    "buffer_load_dwordx4 a[92:95], v1, s[8:11], s72 offen offset:1024 sc1\n"
    "v_mfma_f32_16x16x32_bf16 a[96:99], v[144:147], v[216:219], a[96:99]\n"
    "v_exp_f32 v7, v93\n"
    "v_mfma_f32_16x16x32_bf16 a[100:103], v[148:151], v[216:219], a[100:103]\n"
    "v_cvt_pk_bf16_f32 v141, v4, v5\n"
    "v_mfma_f32_16x16x32_bf16 a[104:107], v[152:155], v[216:219], a[104:107]\n"
    "v_exp_f32 v2, v94\n"
    "v_mfma_f32_16x16x32_bf16 v[184:187], v[156:159], v[216:219], v[184:187]\n"
    "buffer_load_dwordx4 a[108:111], v1, s[8:11], s72 offen offset:2048 sc1\n"
    "v_mfma_f32_16x16x32_bf16 a[112:115], v[144:147], v[220:223], a[112:115]\n"
    "v_exp_f32 v3, v95\n"
    "v_mfma_f32_16x16x32_bf16 a[116:119], v[148:151], v[220:223], a[116:119]\n"
    "v_cvt_pk_bf16_f32 v142, v6, v7\n"
    "v_mfma_f32_16x16x32_bf16 a[120:123], v[152:155], v[220:223], a[120:123]\n"
    "v_cvt_pk_bf16_f32 v143, v2, v3\n"
    "v_mfma_f32_16x16x32_bf16 v[188:191], v[156:159], v[220:223], v[188:191]\n"
    "buffer_load_dwordx4 a[124:127], v1, s[8:11], s72 offen offset:3072 sc1\n"
    "s_waitcnt lgkmcnt(0)\n"
    "s_cbranch_vccnz .Lf_STUB3\n"
    ".Lf_BACK3:\n"
    "s_cbranch_vccnz .Lf_DSK3\n"
    "s_call_b64 s[76:77], .Lf_PROC1\n"
    "buffer_store_dwordx4 v[252:255], v58, s[56:59], s80 offen sc1\n"
    "s_waitcnt lgkmcnt(0)\n"
    "buffer_store_dwordx4 v[248:251], v30, s[56:59], s80 offen sc1\n"
    "s_add_u32 s80, s80, 0x20000\n"
    ".Lf_DBK3:\n"
    "v_mfma_f32_16x16x32_bf16 v[10:13], v[60:63], v[128:131], v[10:13]\n"
    "v_exp_f32 v2, v96\n"
    "v_mfma_f32_16x16x32_bf16 v[14:17], v[60:63], v[132:135], v[14:17]\n"
    "v_exp_f32 v3, v97\n"
    "v_mfma_f32_16x16x32_bf16 v[40:43], v[60:63], v[136:139], v[40:43]\n"
    "v_exp_f32 v4, v98\n"
    "v_mfma_f32_16x16x32_bf16 v[44:47], v[60:63], v[140:143], v[44:47]\n"
    "v_cvt_pk_bf16_f32 v144, v2, v3\n"
    "s_waitcnt vmcnt(14)\n"
    "v_mfma_f32_16x16x32_bf16 v[64:67], a[224:227], a[128:131], v[32:35]\n"
    "v_exp_f32 v5, v99\n"
    "v_mfma_f32_16x16x32_bf16 v[64:67], a[228:231], a[132:135], v[64:67]\n"
    "v_cvt_pk_bf16_f32 v145, v4, v5\n"
    "v_mfma_f32_16x16x32_bf16 v[64:67], a[232:235], a[136:139], v[64:67]\n"
    "v_exp_f32 v6, v112\n"
    "v_mfma_f32_16x16x32_bf16 v[64:67], a[236:239], a[140:143], v[64:67]\n"
    "s_add_u32 s72, s72, 0x8000\n"
    "buffer_load_dwordx4 a[192:195], v0, s[56:59], s72 offen offset:0 sc1\n"
    "v_mfma_f32_16x16x32_bf16 v[68:71], a[224:227], a[144:147], v[32:35]\n"
    "v_mfma_f32_16x16x32_bf16 v[68:71], a[228:231], a[148:151], v[68:71]\n"
    "v_exp_f32 v7, v113\n"
    "v_mfma_f32_16x16x32_bf16 v[68:71], a[232:235], a[152:155], v[68:71]\n"
    "v_exp_f32 v2, v114\n"
    "v_mfma_f32_16x16x32_bf16 v[68:71], a[236:239], a[156:159], v[68:71]\n"
    "buffer_load_dwordx4 a[196:199], v0, s[56:59], s72 offen offset:1024 sc1\n"
    "v_mfma_f32_16x16x32_bf16 v[72:75], a[224:227], a[160:163], v[32:35]\n"
    "v_exp_f32 v3, v115\n"
    "v_mfma_f32_16x16x32_bf16 v[72:75], a[228:231], a[164:167], v[72:75]\n"
    "v_cvt_pk_bf16_f32 v146, v6, v7\n"
    "v_mfma_f32_16x16x32_bf16 v[72:75], a[232:235], a[168:171], v[72:75]\n"
    "v_exp_f32 v4, v100\n"
    "v_mfma_f32_16x16x32_bf16 v[72:75], a[236:239], a[172:175], v[72:75]\n"
    "buffer_load_dwordx4 a[200:203], v0, s[56:59], s72 offen offset:2048 sc1\n"
    "v_mfma_f32_16x16x32_bf16 v[76:79], a[224:227], a[176:179], v[32:35]\n"
    "v_exp_f32 v5, v101\n"
    "v_mfma_f32_16x16x32_bf16 v[76:79], a[228:231], a[180:183], v[76:79]\n"
    "v_cvt_pk_bf16_f32 v147, v2, v3\n"
    "v_mfma_f32_16x16x32_bf16 v[76:79], a[232:235], a[184:187], v[76:79]\n"
    "v_cvt_pk_bf16_f32 v148, v4, v5\n"
    "v_mfma_f32_16x16x32_bf16 v[76:79], a[236:239], a[188:191], v[76:79]\n"
    "buffer_load_dwordx4 a[204:207], v0, s[56:59], s72 offen offset:3072 sc1\n"
    "s_waitcnt vmcnt(14)\n"
    "v_mfma_f32_16x16x32_bf16 v[80:83], a[240:243], a[128:131], v[36:39]\n"
    "v_exp_f32 v6, v102\n"
    "v_mfma_f32_16x16x32_bf16 v[80:83], a[244:247], a[132:135], v[80:83]\n"
    "v_exp_f32 v7, v103\n"
    "v_mfma_f32_16x16x32_bf16 v[80:83], a[248:251], a[136:139], v[80:83]\n"
    "v_cvt_pk_bf16_f32 v149, v6, v7\n"
    "v_mfma_f32_16x16x32_bf16 v[80:83], a[252:255], a[140:143], v[80:83]\n"
    "buffer_load_dwordx4 a[208:211], v1, s[56:59], s72 offen offset:0 sc1\n"
    "v_mfma_f32_16x16x32_bf16 v[84:87], a[240:243], a[144:147], v[36:39]\n"
    "v_exp_f32 v2, v116\n"
    "v_mfma_f32_16x16x32_bf16 v[84:87], a[244:247], a[148:151], v[84:87]\n"
    "v_exp_f32 v3, v117\n"
    "v_mfma_f32_16x16x32_bf16 v[84:87], a[248:251], a[152:155], v[84:87]\n"
    "v_cvt_pk_bf16_f32 v150, v2, v3\n"
    "v_mfma_f32_16x16x32_bf16 v[84:87], a[252:255], a[156:159], v[84:87]\n"
    "buffer_load_dwordx4 a[212:215], v1, s[56:59], s72 offen offset:1024 sc1\n"
    "v_mfma_f32_16x16x32_bf16 v[88:91], a[240:243], a[160:163], v[36:39]\n"
    "v_exp_f32 v4, v118\n"
    "v_mfma_f32_16x16x32_bf16 v[88:91], a[244:247], a[164:167], v[88:91]\n"
    "v_exp_f32 v5, v119\n"
    "v_mfma_f32_16x16x32_bf16 v[88:91], a[248:251], a[168:171], v[88:91]\n"
    "v_cvt_pk_bf16_f32 v151, v4, v5\n"
    "v_mfma_f32_16x16x32_bf16 v[88:91], a[252:255], a[172:175], v[88:91]\n"
    "buffer_load_dwordx4 a[216:219], v1, s[56:59], s72 offen offset:2048 sc1\n"
    "v_mfma_f32_16x16x32_bf16 v[92:95], a[240:243], a[176:179], v[36:39]\n"
    "v_exp_f32 v6, v104\n"
    "v_mfma_f32_16x16x32_bf16 v[92:95], a[244:247], a[180:183], v[92:95]\n"
    "v_exp_f32 v7, v105\n"
    "v_mfma_f32_16x16x32_bf16 v[92:95], a[248:251], a[184:187], v[92:95]\n"
    "v_cvt_pk_bf16_f32 v152, v6, v7\n"
    "v_mfma_f32_16x16x32_bf16 v[92:95], a[252:255], a[188:191], v[92:95]\n"
    "buffer_load_dwordx4 a[220:223], v1, s[56:59], s72 offen offset:3072 sc1\n"
    "s_waitcnt vmcnt(14)\n"
    "v_mfma_f32_16x16x32_bf16 a[0:3], v[128:131], a[12:15], a[0:3]\n"
    "v_exp_f32 v2, v106\n"
    "v_mfma_f32_16x16x32_bf16 a[4:7], v[132:135], a[12:15], a[4:7]\n"
    "v_mfma_f32_16x16x32_bf16 a[8:11], v[136:139], a[12:15], a[8:11]\n"
    "v_exp_f32 v3, v107\n"
    "v_mfma_f32_16x16x32_bf16 v[160:163], v[140:143], a[12:15], v[160:163]\n"
    "buffer_load_dwordx4 v[192:195], v0, s[8:11], s72 offen offset:0 sc1\n"
    "v_cvt_pk_bf16_f32 v153, v2, v3\n"
    "v_mfma_f32_16x16x32_bf16 a[16:19], v[128:131], a[28:31], a[16:19]\n"
    "v_exp_f32 v4, v120\n"
    "v_mfma_f32_16x16x32_bf16 a[20:23], v[132:135], a[28:31], a[20:23]\n"
    "ds_read_b128 v[32:35], v9 offset:960\n"
    "v_mfma_f32_16x16x32_bf16 a[24:27], v[136:139], a[28:31], a[24:27]\n"
    "v_exp_f32 v5, v121\n"
    "v_mfma_f32_16x16x32_bf16 v[164:167], v[140:143], a[28:31], v[164:167]\n"
    "buffer_load_dwordx4 v[196:199], v0, s[8:11], s72 offen offset:1024 sc1\n"
    "v_mfma_f32_16x16x32_bf16 a[32:35], v[128:131], a[44:47], a[32:35]\n"
    "v_exp_f32 v6, v122\n"
    "v_mfma_f32_16x16x32_bf16 a[36:39], v[132:135], a[44:47], a[36:39]\n"
    "ds_read_b128 v[36:39], v9 offset:976\n"
    "v_mfma_f32_16x16x32_bf16 a[40:43], v[136:139], a[44:47], a[40:43]\n"
    "v_exp_f32 v7, v123\n"
    "v_mfma_f32_16x16x32_bf16 v[168:171], v[140:143], a[44:47], v[168:171]\n"
    "buffer_load_dwordx4 v[200:203], v0, s[8:11], s72 offen offset:2048 sc1\n"
    "v_mfma_f32_16x16x32_bf16 a[48:51], v[128:131], a[60:63], a[48:51]\n"
    "v_cvt_pk_bf16_f32 v154, v4, v5\n"
    "v_cvt_pk_bf16_f32 v155, v6, v7\n"
    "v_mfma_f32_16x16x32_bf16 a[52:55], v[132:135], a[60:63], a[52:55]\n"
    "ds_read_b128 v[60:63], v9 offset:608\n"
    "v_mfma_f32_16x16x32_bf16 a[56:59], v[136:139], a[60:63], a[56:59]\n"
    "v_exp_f32 v2, v108\n"
    "v_mfma_f32_16x16x32_bf16 v[172:175], v[140:143], a[60:63], v[172:175]\n"
    "buffer_load_dwordx4 v[204:207], v0, s[8:11], s72 offen offset:3072 sc1\n"
    "s_waitcnt vmcnt(14)\n"
    "v_mfma_f32_16x16x32_bf16 a[64:67], v[128:131], a[76:79], a[64:67]\n"
    "v_mfma_f32_16x16x32_bf16 a[68:71], v[132:135], a[76:79], a[68:71]\n"
    "v_exp_f32 v3, v109\n"
    "v_mfma_f32_16x16x32_bf16 a[72:75], v[136:139], a[76:79], a[72:75]\n"
    "v_exp_f32 v4, v110\n"
    "v_mfma_f32_16x16x32_bf16 v[176:179], v[140:143], a[76:79], v[176:179]\n"
    "buffer_load_dwordx4 v[208:211], v1, s[8:11], s72 offen offset:0 sc1\n"
    "v_cvt_pk_bf16_f32 v156, v2, v3\n"
    "v_mfma_f32_16x16x32_bf16 a[80:83], v[128:131], a[92:95], a[80:83]\n"
    "v_mfma_f32_16x16x32_bf16 a[84:87], v[132:135], a[92:95], a[84:87]\n"
    "v_exp_f32 v5, v111\n"
    "v_mfma_f32_16x16x32_bf16 a[88:91], v[136:139], a[92:95], a[88:91]\n"
    "v_exp_f32 v6, v124\n"
    "v_mfma_f32_16x16x32_bf16 v[180:183], v[140:143], a[92:95], v[180:183]\n"
    "buffer_load_dwordx4 v[212:215], v1, s[8:11], s72 offen offset:1024 sc1\n"
    "v_cvt_pk_bf16_f32 v157, v4, v5\n"
    "v_mfma_f32_16x16x32_bf16 a[96:99], v[128:131], a[108:111], a[96:99]\n"
    "v_mfma_f32_16x16x32_bf16 a[100:103], v[132:135], a[108:111], a[100:103]\n"
    "v_exp_f32 v7, v125\n"
    "v_mfma_f32_16x16x32_bf16 a[104:107], v[136:139], a[108:111], a[104:107]\n"
    "v_exp_f32 v2, v126\n"
    "v_mfma_f32_16x16x32_bf16 v[184:187], v[140:143], a[108:111], v[184:187]\n"
    "buffer_load_dwordx4 v[216:219], v1, s[8:11], s72 offen offset:2048 sc1\n"
    "v_cvt_pk_bf16_f32 v158, v6, v7\n"
    "v_mfma_f32_16x16x32_bf16 a[112:115], v[128:131], a[124:127], a[112:115]\n"
    "v_mfma_f32_16x16x32_bf16 a[116:119], v[132:135], a[124:127], a[116:119]\n"
    "v_exp_f32 v3, v127\n"
    "v_mfma_f32_16x16x32_bf16 a[120:123], v[136:139], a[124:127], a[120:123]\n"
    "v_cvt_pk_bf16_f32 v159, v2, v3\n"
    "v_mfma_f32_16x16x32_bf16 v[188:191], v[140:143], a[124:127], v[188:191]\n"
    "buffer_load_dwordx4 v[220:223], v1, s[8:11], s72 offen offset:3072 sc1\n"
    "s_waitcnt lgkmcnt(0)\n"
    "s_cbranch_vccnz .Lf_STUB4\n"
    ".Lf_BACK4:\n"
    "v_mfma_f32_16x16x32_bf16 v[10:13], v[60:63], v[144:147], v[10:13]\n"
    "v_exp_f32 v2, v64\n"
    "v_mfma_f32_16x16x32_bf16 v[14:17], v[60:63], v[148:151], v[14:17]\n"
    "v_exp_f32 v3, v65\n"
    "v_mfma_f32_16x16x32_bf16 v[40:43], v[60:63], v[152:155], v[40:43]\n"
    "v_exp_f32 v4, v66\n"
    "v_mfma_f32_16x16x32_bf16 v[44:47], v[60:63], v[156:159], v[44:47]\n"
    "v_exp_f32 v5, v67\n"
    "s_waitcnt vmcnt(12)\n"
    "v_mfma_f32_16x16x32_bf16 v[96:99], a[192:195], a[128:131], v[32:35]\n"
    "v_cvt_pk_bf16_f32 v128, v2, v3\n"
    "v_cvt_pk_bf16_f32 v129, v4, v5\n"
    "v_mfma_f32_16x16x32_bf16 v[96:99], a[196:199], a[132:135], v[96:99]\n"
    "v_mfma_f32_16x16x32_bf16 v[96:99], a[200:203], a[136:139], v[96:99]\n"
    "v_exp_f32 v6, v80\n"
    "v_mfma_f32_16x16x32_bf16 v[96:99], a[204:207], a[140:143], v[96:99]\n"
    "s_add_u32 s72, s72, 0x8000\n"
    "buffer_load_dwordx4 a[224:227], v0, s[56:59], s72 offen offset:0 sc1\n"
    "v_mfma_f32_16x16x32_bf16 v[100:103], a[192:195], a[144:147], v[32:35]\n"
    "s_waitcnt vmcnt(17)\n"
    "v_mov_b32 v240, 126988\n"
    "v_mov_b32 v241, 1\n"
    "s_mov_b64 exec, 1\n"
    "ds_add_rtn_u32 v240, v240, v241\n"
    "s_mov_b64 exec, -1\n"
    "v_mfma_f32_16x16x32_bf16 v[100:103], a[196:199], a[148:151], v[100:103]\n"
    "v_mfma_f32_16x16x32_bf16 v[100:103], a[200:203], a[152:155], v[100:103]\n"
    "v_mfma_f32_16x16x32_bf16 v[100:103], a[204:207], a[156:159], v[100:103]\n"
    "buffer_load_dwordx4 a[228:231], v0, s[56:59], s72 offen offset:1024 sc1\n"
    "v_mfma_f32_16x16x32_bf16 v[104:107], a[192:195], a[160:163], v[32:35]\n"
    "v_exp_f32 v7, v81\n"
    "v_mfma_f32_16x16x32_bf16 v[104:107], a[196:199], a[164:167], v[104:107]\n"
    "v_exp_f32 v2, v82\n"
    "v_mfma_f32_16x16x32_bf16 v[104:107], a[200:203], a[168:171], v[104:107]\n"
    "v_exp_f32 v3, v83\n"
    "v_mfma_f32_16x16x32_bf16 v[104:107], a[204:207], a[172:175], v[104:107]\n"
    "buffer_load_dwordx4 a[232:235], v0, s[56:59], s72 offen offset:2048 sc1\n"
    "v_mfma_f32_16x16x32_bf16 v[108:111], a[192:195], a[176:179], v[32:35]\n"
    "s_waitcnt lgkmcnt(0)\n"
    "v_readfirstlane_b32 s88, v240\n"
    "s_cmp_eq_u32 s88, 3\n"
    "s_cbranch_scc1 .Lf_FSTUB3\n"
    ".Lf_FBACK3:\n"
    "v_mfma_f32_16x16x32_bf16 v[108:111], a[196:199], a[180:183], v[108:111]\n"
    "v_mfma_f32_16x16x32_bf16 v[108:111], a[200:203], a[184:187], v[108:111]\n"
    "v_cvt_pk_bf16_f32 v130, v6, v7\n"
    "v_mfma_f32_16x16x32_bf16 v[108:111], a[204:207], a[188:191], v[108:111]\n"
    "buffer_load_dwordx4 a[236:239], v0, s[56:59], s72 offen offset:3072 sc1\n"
    "v_cvt_pk_bf16_f32 v131, v2, v3\n"
    "s_waitcnt vmcnt(12)\n"
    "v_mfma_f32_16x16x32_bf16 v[112:115], a[208:211], a[128:131], v[36:39]\n"
    "v_mfma_f32_16x16x32_bf16 v[112:115], a[212:215], a[132:135], v[112:115]\n"
    "v_exp_f32 v4, v68\n"
    "v_mfma_f32_16x16x32_bf16 v[112:115], a[216:219], a[136:139], v[112:115]\n"
    "v_exp_f32 v5, v69\n"
    "v_mfma_f32_16x16x32_bf16 v[112:115], a[220:223], a[140:143], v[112:115]\n"
    "buffer_load_dwordx4 a[240:243], v1, s[56:59], s72 offen offset:0 sc1\n"
    "v_cvt_pk_bf16_f32 v132, v4, v5\n"
    "v_mfma_f32_16x16x32_bf16 v[116:119], a[208:211], a[144:147], v[36:39]\n"
    "v_exp_f32 v6, v70\n"
    "v_mfma_f32_16x16x32_bf16 v[116:119], a[212:215], a[148:151], v[116:119]\n"
    "v_mfma_f32_16x16x32_bf16 v[116:119], a[216:219], a[152:155], v[116:119]\n"
    "v_exp_f32 v7, v71\n"
    "v_mfma_f32_16x16x32_bf16 v[116:119], a[220:223], a[156:159], v[116:119]\n"
    "buffer_load_dwordx4 a[244:247], v1, s[56:59], s72 offen offset:1024 sc1\n"
    "v_cvt_pk_bf16_f32 v133, v6, v7\n"
    "v_mfma_f32_16x16x32_bf16 v[120:123], a[208:211], a[160:163], v[36:39]\n"
    "v_exp_f32 v2, v84\n"
    "v_mfma_f32_16x16x32_bf16 v[120:123], a[212:215], a[164:167], v[120:123]\n"
    "v_exp_f32 v3, v85\n"
    "v_mfma_f32_16x16x32_bf16 v[120:123], a[216:219], a[168:171], v[120:123]\n"
    "v_cvt_pk_bf16_f32 v134, v2, v3\n"
    "v_mfma_f32_16x16x32_bf16 v[120:123], a[220:223], a[172:175], v[120:123]\n"
    "buffer_load_dwordx4 a[248:251], v1, s[56:59], s72 offen offset:2048 sc1\n"
    "v_mfma_f32_16x16x32_bf16 v[124:127], a[208:211], a[176:179], v[36:39]\n"
    "v_exp_f32 v4, v86\n"
    "v_mfma_f32_16x16x32_bf16 v[124:127], a[212:215], a[180:183], v[124:127]\n"
    "v_exp_f32 v5, v87\n"
    "v_mfma_f32_16x16x32_bf16 v[124:127], a[216:219], a[184:187], v[124:127]\n"
    "v_exp_f32 v6, v72\n"
    "v_mfma_f32_16x16x32_bf16 v[124:127], a[220:223], a[188:191], v[124:127]\n"
    "buffer_load_dwordx4 a[252:255], v1, s[56:59], s72 offen offset:3072 sc1\n"
    "s_waitcnt vmcnt(12)\n"
    "v_mfma_f32_16x16x32_bf16 a[0:3], v[144:147], v[192:195], a[0:3]\n"
    "v_exp_f32 v7, v73\n"
    "v_mfma_f32_16x16x32_bf16 a[4:7], v[148:151], v[192:195], a[4:7]\n"
    "v_cvt_pk_bf16_f32 v135, v4, v5\n"
    "v_mfma_f32_16x16x32_bf16 a[8:11], v[152:155], v[192:195], a[8:11]\n"
    "v_exp_f32 v2, v74\n"
    "v_mfma_f32_16x16x32_bf16 v[160:163], v[156:159], v[192:195], v[160:163]\n"
    "buffer_load_dwordx4 a[12:15], v0, s[8:11], s72 offen offset:0 sc1\n"
    "v_cvt_pk_bf16_f32 v136, v6, v7\n"
    "v_mfma_f32_16x16x32_bf16 a[16:19], v[144:147], v[196:199], a[16:19]\n"
    "v_mfma_f32_16x16x32_bf16 a[20:23], v[148:151], v[196:199], a[20:23]\n"
    "ds_read_b128 v[32:35], v9 offset:1152\n"
    "v_mfma_f32_16x16x32_bf16 a[24:27], v[152:155], v[196:199], a[24:27]\n"
    "v_exp_f32 v3, v75\n"
    "v_mfma_f32_16x16x32_bf16 v[164:167], v[156:159], v[196:199], v[164:167]\n"
    "buffer_load_dwordx4 a[28:31], v0, s[8:11], s72 offen offset:1024 sc1\n"
    "v_cvt_pk_bf16_f32 v137, v2, v3\n"
    "v_mfma_f32_16x16x32_bf16 a[32:35], v[144:147], v[200:203], a[32:35]\n"
    "v_exp_f32 v4, v88\n"
    "v_mfma_f32_16x16x32_bf16 a[36:39], v[148:151], v[200:203], a[36:39]\n"
    "ds_read_b128 v[36:39], v9 offset:1168\n"
    "v_mfma_f32_16x16x32_bf16 a[40:43], v[152:155], v[200:203], a[40:43]\n"
    "v_exp_f32 v5, v89\n"
    "v_mfma_f32_16x16x32_bf16 v[168:171], v[156:159], v[200:203], v[168:171]\n"
    "buffer_load_dwordx4 a[44:47], v0, s[8:11], s72 offen offset:2048 sc1\n"
    "v_cvt_pk_bf16_f32 v138, v4, v5\n"
    "v_mfma_f32_16x16x32_bf16 a[48:51], v[144:147], v[204:207], a[48:51]\n"
    "v_exp_f32 v6, v90\n"
    "v_mfma_f32_16x16x32_bf16 a[52:55], v[148:151], v[204:207], a[52:55]\n"
    "ds_read_b128 v[60:63], v9 offset:800\n"
    "v_mfma_f32_16x16x32_bf16 a[56:59], v[152:155], v[204:207], a[56:59]\n"
    "v_exp_f32 v7, v91\n"
    "v_mfma_f32_16x16x32_bf16 v[172:175], v[156:159], v[204:207], v[172:175]\n"
    "buffer_load_dwordx4 a[60:63], v0, s[8:11], s72 offen offset:3072 sc1\n"
    "v_cvt_pk_bf16_f32 v139, v6, v7\n"
    "s_waitcnt vmcnt(12)\n"
    "v_mfma_f32_16x16x32_bf16 a[64:67], v[144:147], v[208:211], a[64:67]\n"
    "v_mfma_f32_16x16x32_bf16 a[68:71], v[148:151], v[208:211], a[68:71]\n"
    "v_exp_f32 v2, v76\n"
    "v_mfma_f32_16x16x32_bf16 a[72:75], v[152:155], v[208:211], a[72:75]\n"
    "v_exp_f32 v3, v77\n"
    "v_mfma_f32_16x16x32_bf16 v[176:179], v[156:159], v[208:211], v[176:179]\n"
    "buffer_load_dwordx4 a[76:79], v1, s[8:11], s72 offen offset:0 sc1\n"
    "v_cvt_pk_bf16_f32 v140, v2, v3\n"
    "v_mfma_f32_16x16x32_bf16 a[80:83], v[144:147], v[212:215], a[80:83]\n"
    "v_exp_f32 v4, v78\n"
    "v_mfma_f32_16x16x32_bf16 a[84:87], v[148:151], v[212:215], a[84:87]\n"
    "v_mfma_f32_16x16x32_bf16 a[88:91], v[152:155], v[212:215], a[88:91]\n"
    "v_exp_f32 v5, v79\n"
    "v_mfma_f32_16x16x32_bf16 v[180:183], v[156:159], v[212:215], v[180:183]\n"
    "buffer_load_dwordx4 a[92:95], v1, s[8:11], s72 offen offset:1024 sc1\n"
    "v_cvt_pk_bf16_f32 v141, v4, v5\n"
    "v_mfma_f32_16x16x32_bf16 a[96:99], v[144:147], v[216:219], a[96:99]\n"
    "v_exp_f32 v6, v92\n"
    "v_mfma_f32_16x16x32_bf16 a[100:103], v[148:151], v[216:219], a[100:103]\n"
    "v_exp_f32 v7, v93\n"
    "v_mfma_f32_16x16x32_bf16 a[104:107], v[152:155], v[216:219], a[104:107]\n"
    "v_cvt_pk_bf16_f32 v142, v6, v7\n"
    "v_mfma_f32_16x16x32_bf16 v[184:187], v[156:159], v[216:219], v[184:187]\n"
    "buffer_load_dwordx4 a[108:111], v1, s[8:11], s72 offen offset:2048 sc1\n"
    "v_mfma_f32_16x16x32_bf16 a[112:115], v[144:147], v[220:223], a[112:115]\n"
    "v_exp_f32 v2, v94\n"
    "v_mfma_f32_16x16x32_bf16 a[116:119], v[148:151], v[220:223], a[116:119]\n"
    "v_exp_f32 v3, v95\n"
    "v_mfma_f32_16x16x32_bf16 a[120:123], v[152:155], v[220:223], a[120:123]\n"
    "v_cvt_pk_bf16_f32 v143, v2, v3\n"
    "v_mfma_f32_16x16x32_bf16 v[188:191], v[156:159], v[220:223], v[188:191]\n"
    "buffer_load_dwordx4 a[124:127], v1, s[8:11], s72 offen offset:3072 sc1\n"
    "s_waitcnt lgkmcnt(0)\n"
    "s_cbranch_vccnz .Lf_STUB5\n"
    ".Lf_BACK5:\n"
    "v_mfma_f32_16x16x32_bf16 v[10:13], v[60:63], v[128:131], v[10:13]\n"
    "v_exp_f32 v2, v96\n"
    "v_mfma_f32_16x16x32_bf16 v[14:17], v[60:63], v[132:135], v[14:17]\n"
    "v_exp_f32 v3, v97\n"
    "v_mfma_f32_16x16x32_bf16 v[40:43], v[60:63], v[136:139], v[40:43]\n"
    "v_exp_f32 v4, v98\n"
    "v_mfma_f32_16x16x32_bf16 v[44:47], v[60:63], v[140:143], v[44:47]\n"
    "v_exp_f32 v5, v99\n"
    "s_waitcnt vmcnt(12)\n"
    "v_mfma_f32_16x16x32_bf16 v[64:67], a[224:227], a[128:131], v[32:35]\n"
    "v_cvt_pk_bf16_f32 v144, v2, v3\n"
    "v_mfma_f32_16x16x32_bf16 v[64:67], a[228:231], a[132:135], v[64:67]\n"
    "v_exp_f32 v6, v112\n"
    "v_mfma_f32_16x16x32_bf16 v[64:67], a[232:235], a[136:139], v[64:67]\n"
    "v_cvt_pk_bf16_f32 v145, v4, v5\n"
    "v_mfma_f32_16x16x32_bf16 v[64:67], a[236:239], a[140:143], v[64:67]\n"
    "s_add_u32 s72, s72, 0x8000\n"
    "buffer_load_dwordx4 a[192:195], v0, s[56:59], s72 offen offset:0 sc1\n"
    "v_mfma_f32_16x16x32_bf16 v[68:71], a[224:227], a[144:147], v[32:35]\n"
    "v_mfma_f32_16x16x32_bf16 v[68:71], a[228:231], a[148:151], v[68:71]\n"
    "v_exp_f32 v7, v113\n"
    "v_mfma_f32_16x16x32_bf16 v[68:71], a[232:235], a[152:155], v[68:71]\n"
    "v_exp_f32 v2, v114\n"
    "v_mfma_f32_16x16x32_bf16 v[68:71], a[236:239], a[156:159], v[68:71]\n"
    "buffer_load_dwordx4 a[196:199], v0, s[56:59], s72 offen offset:1024 sc1\n"
    "v_cvt_pk_bf16_f32 v146, v6, v7\n"
    "v_mfma_f32_16x16x32_bf16 v[72:75], a[224:227], a[160:163], v[32:35]\n"
    "v_mfma_f32_16x16x32_bf16 v[72:75], a[228:231], a[164:167], v[72:75]\n"
    "v_exp_f32 v3, v115\n"
    "v_mfma_f32_16x16x32_bf16 v[72:75], a[232:235], a[168:171], v[72:75]\n"
    "v_exp_f32 v4, v100\n"
    "v_mfma_f32_16x16x32_bf16 v[72:75], a[236:239], a[172:175], v[72:75]\n"
    "buffer_load_dwordx4 a[200:203], v0, s[56:59], s72 offen offset:2048 sc1\n"
    "v_cvt_pk_bf16_f32 v147, v2, v3\n"
    "v_mfma_f32_16x16x32_bf16 v[76:79], a[224:227], a[176:179], v[32:35]\n"
    "v_exp_f32 v5, v101\n"
    "v_mfma_f32_16x16x32_bf16 v[76:79], a[228:231], a[180:183], v[76:79]\n"
    "v_cvt_pk_bf16_f32 v148, v4, v5\n"
    "v_mfma_f32_16x16x32_bf16 v[76:79], a[232:235], a[184:187], v[76:79]\n"
    "v_exp_f32 v6, v102\n"
    "v_mfma_f32_16x16x32_bf16 v[76:79], a[236:239], a[188:191], v[76:79]\n"
    "buffer_load_dwordx4 a[204:207], v0, s[56:59], s72 offen offset:3072 sc1\n"
    "s_waitcnt vmcnt(12)\n"
    "v_mfma_f32_16x16x32_bf16 v[80:83], a[240:243], a[128:131], v[36:39]\n"
    "v_exp_f32 v7, v103\n"
    "v_mfma_f32_16x16x32_bf16 v[80:83], a[244:247], a[132:135], v[80:83]\n"
    "v_cvt_pk_bf16_f32 v149, v6, v7\n"
    "v_mfma_f32_16x16x32_bf16 v[80:83], a[248:251], a[136:139], v[80:83]\n"
    "v_exp_f32 v2, v116\n"
    "v_mfma_f32_16x16x32_bf16 v[80:83], a[252:255], a[140:143], v[80:83]\n"
    "buffer_load_dwordx4 a[208:211], v1, s[56:59], s72 offen offset:0 sc1\n"
    "v_mfma_f32_16x16x32_bf16 v[84:87], a[240:243], a[144:147], v[36:39]\n"
    "v_exp_f32 v3, v117\n"
    "v_mfma_f32_16x16x32_bf16 v[84:87], a[244:247], a[148:151], v[84:87]\n"
    "v_cvt_pk_bf16_f32 v150, v2, v3\n"
    "v_mfma_f32_16x16x32_bf16 v[84:87], a[248:251], a[152:155], v[84:87]\n"
    "v_exp_f32 v4, v118\n"
    "v_mfma_f32_16x16x32_bf16 v[84:87], a[252:255], a[156:159], v[84:87]\n"
    "buffer_load_dwordx4 a[212:215], v1, s[56:59], s72 offen offset:1024 sc1\n"
    "v_mfma_f32_16x16x32_bf16 v[88:91], a[240:243], a[160:163], v[36:39]\n"
    "v_exp_f32 v5, v119\n"
    "v_mfma_f32_16x16x32_bf16 v[88:91], a[244:247], a[164:167], v[88:91]\n"
    "v_cvt_pk_bf16_f32 v151, v4, v5\n"
    "v_mfma_f32_16x16x32_bf16 v[88:91], a[248:251], a[168:171], v[88:91]\n"
    "v_exp_f32 v6, v104\n"
    "v_mfma_f32_16x16x32_bf16 v[88:91], a[252:255], a[172:175], v[88:91]\n"
    "buffer_load_dwordx4 a[216:219], v1, s[56:59], s72 offen offset:2048 sc1\n"
    "v_mfma_f32_16x16x32_bf16 v[92:95], a[240:243], a[176:179], v[36:39]\n"
    "v_exp_f32 v7, v105\n"
    "v_mfma_f32_16x16x32_bf16 v[92:95], a[244:247], a[180:183], v[92:95]\n"
    "v_exp_f32 v2, v106\n"
    "v_mfma_f32_16x16x32_bf16 v[92:95], a[248:251], a[184:187], v[92:95]\n"
    "v_cvt_pk_bf16_f32 v152, v6, v7\n"
    "v_mfma_f32_16x16x32_bf16 v[92:95], a[252:255], a[188:191], v[92:95]\n"
    "buffer_load_dwordx4 a[220:223], v1, s[56:59], s72 offen offset:3072 sc1\n"
    "v_add_u32 v24, 0x8000, v24\n"
    "s_mov_b64 exec, 0xffff\n"
    "buffer_load_dwordx2 v[22:23], v24, s[56:59], 0 offen sc1\n"
    "s_mov_b64 exec, -1\n"
    "s_waitcnt vmcnt(13)\n"
    "v_mfma_f32_16x16x32_bf16 a[0:3], v[128:131], a[12:15], a[0:3]\n"
    "v_mfma_f32_16x16x32_bf16 a[4:7], v[132:135], a[12:15], a[4:7]\n"
    "v_mfma_f32_16x16x32_bf16 a[8:11], v[136:139], a[12:15], a[8:11]\n"
    "v_mfma_f32_16x16x32_bf16 v[160:163], v[140:143], a[12:15], v[160:163]\n"
    "buffer_load_dwordx4 v[192:195], v0, s[8:11], s72 offen offset:0 sc1\n"
    "v_mfma_f32_16x16x32_bf16 a[16:19], v[128:131], a[28:31], a[16:19]\n"
    "v_exp_f32 v3, v107\n"
    "v_mfma_f32_16x16x32_bf16 a[20:23], v[132:135], a[28:31], a[20:23]\n"
    "ds_read_b128 v[32:35], v9 offset:1344\n"
    "v_cvt_pk_bf16_f32 v153, v2, v3\n"
    "v_mfma_f32_16x16x32_bf16 a[24:27], v[136:139], a[28:31], a[24:27]\n"
    "v_exp_f32 v4, v120\n"
    "v_mfma_f32_16x16x32_bf16 v[164:167], v[140:143], a[28:31], v[164:167]\n"
    "buffer_load_dwordx4 v[196:199], v0, s[8:11], s72 offen offset:1024 sc1\n"
    "v_mfma_f32_16x16x32_bf16 a[32:35], v[128:131], a[44:47], a[32:35]\n"
    "v_exp_f32 v5, v121\n"
    "v_mfma_f32_16x16x32_bf16 a[36:39], v[132:135], a[44:47], a[36:39]\n"
    "ds_read_b128 v[36:39], v9 offset:1360\n"
    "v_cvt_pk_bf16_f32 v154, v4, v5\n"
    "v_mfma_f32_16x16x32_bf16 a[40:43], v[136:139], a[44:47], a[40:43]\n"
    "v_mfma_f32_16x16x32_bf16 v[168:171], v[140:143], a[44:47], v[168:171]\n"
    "buffer_load_dwordx4 v[200:203], v0, s[8:11], s72 offen offset:2048 sc1\n"
    "v_mfma_f32_16x16x32_bf16 a[48:51], v[128:131], a[60:63], a[48:51]\n"
    "v_exp_f32 v6, v122\n"
    "v_mfma_f32_16x16x32_bf16 a[52:55], v[132:135], a[60:63], a[52:55]\n"
    "ds_read_b128 v[60:63], v9 offset:992\n"
    "v_mfma_f32_16x16x32_bf16 a[56:59], v[136:139], a[60:63], a[56:59]\n"
    "v_exp_f32 v7, v123\n"
    "v_mfma_f32_16x16x32_bf16 v[172:175], v[140:143], a[60:63], v[172:175]\n"
    "buffer_load_dwordx4 v[204:207], v0, s[8:11], s72 offen offset:3072 sc1\n"
    "v_cvt_pk_bf16_f32 v155, v6, v7\n"
    "s_waitcnt vmcnt(13)\n"
    "v_mfma_f32_16x16x32_bf16 a[64:67], v[128:131], a[76:79], a[64:67]\n"
    "v_exp_f32 v2, v108\n"
    "v_mfma_f32_16x16x32_bf16 a[68:71], v[132:135], a[76:79], a[68:71]\n"
    "v_exp_f32 v3, v109\n"
    "v_mfma_f32_16x16x32_bf16 a[72:75], v[136:139], a[76:79], a[72:75]\n"
    "v_exp_f32 v4, v110\n"
    "v_mfma_f32_16x16x32_bf16 v[176:179], v[140:143], a[76:79], v[176:179]\n"
    "buffer_load_dwordx4 v[208:211], v1, s[8:11], s72 offen offset:0 sc1\n"
    "v_mfma_f32_16x16x32_bf16 a[80:83], v[128:131], a[92:95], a[80:83]\n"
    "v_exp_f32 v5, v111\n"
    "v_mfma_f32_16x16x32_bf16 a[84:87], v[132:135], a[92:95], a[84:87]\n"
    "v_cvt_pk_bf16_f32 v156, v2, v3\n"
    "v_mfma_f32_16x16x32_bf16 a[88:91], v[136:139], a[92:95], a[88:91]\n"
    "v_exp_f32 v6, v124\n"
    "v_mfma_f32_16x16x32_bf16 v[180:183], v[140:143], a[92:95], v[180:183]\n"
    "buffer_load_dwordx4 v[212:215], v1, s[8:11], s72 offen offset:1024 sc1\n"
    "v_mfma_f32_16x16x32_bf16 a[96:99], v[128:131], a[108:111], a[96:99]\n"
    "v_exp_f32 v7, v125\n"
    "v_mfma_f32_16x16x32_bf16 a[100:103], v[132:135], a[108:111], a[100:103]\n"
    "v_cvt_pk_bf16_f32 v157, v4, v5\n"
    "v_mfma_f32_16x16x32_bf16 a[104:107], v[136:139], a[108:111], a[104:107]\n"
    "v_exp_f32 v2, v126\n"
    "v_mfma_f32_16x16x32_bf16 v[184:187], v[140:143], a[108:111], v[184:187]\n"
    "buffer_load_dwordx4 v[216:219], v1, s[8:11], s72 offen offset:2048 sc1\n"
    "v_mfma_f32_16x16x32_bf16 a[112:115], v[128:131], a[124:127], a[112:115]\n"
    "v_exp_f32 v3, v127\n"
    "v_mfma_f32_16x16x32_bf16 a[116:119], v[132:135], a[124:127], a[116:119]\n"
    "v_cvt_pk_bf16_f32 v158, v6, v7\n"
    "v_mfma_f32_16x16x32_bf16 a[120:123], v[136:139], a[124:127], a[120:123]\n"
    "v_cvt_pk_bf16_f32 v159, v2, v3\n"
    "v_mfma_f32_16x16x32_bf16 v[188:191], v[140:143], a[124:127], v[188:191]\n"
    "buffer_load_dwordx4 v[220:223], v1, s[8:11], s72 offen offset:3072 sc1\n"
    "s_waitcnt lgkmcnt(0)\n"
    "s_cbranch_vccnz .Lf_STUB6\n"
    ".Lf_BACK6:\n"
    "v_mfma_f32_16x16x32_bf16 v[10:13], v[60:63], v[144:147], v[10:13]\n"
    "v_exp_f32 v2, v64\n"
    "v_mfma_f32_16x16x32_bf16 v[14:17], v[60:63], v[148:151], v[14:17]\n"
    "v_exp_f32 v3, v65\n"
    "v_mfma_f32_16x16x32_bf16 v[40:43], v[60:63], v[152:155], v[40:43]\n"
    "v_exp_f32 v4, v66\n"
    "v_mfma_f32_16x16x32_bf16 v[44:47], v[60:63], v[156:159], v[44:47]\n"
    "v_exp_f32 v5, v67\n"
    "s_waitcnt vmcnt(13)\n"
    "v_mfma_f32_16x16x32_bf16 v[96:99], a[192:195], a[128:131], v[32:35]\n"
    "v_cvt_pk_bf16_f32 v128, v2, v3\n"
    "v_mfma_f32_16x16x32_bf16 v[96:99], a[196:199], a[132:135], v[96:99]\n"
    "v_exp_f32 v6, v80\n"
    "v_mfma_f32_16x16x32_bf16 v[96:99], a[200:203], a[136:139], v[96:99]\n"
    "v_cvt_pk_bf16_f32 v129, v4, v5\n"
    "v_mfma_f32_16x16x32_bf16 v[96:99], a[204:207], a[140:143], v[96:99]\n"
    "s_waitcnt vmcnt(8)\n"
    "v_cmp_ne_u64 s[88:89], s[74:75], v[22:23]\n"
    "s_and_b32 s88, s88, 0xffff\n"
    "s_cbranch_scc1 .Lf_PSTUB2\n"
    ".Lf_PBACK2:\n"
    "s_add_u32 s72, s72, 0x8000\n"
    "buffer_load_dwordx4 a[224:227], v0, s[56:59], s72 offen offset:0 sc1\n"
    "v_mfma_f32_16x16x32_bf16 v[100:103], a[192:195], a[144:147], v[32:35]\n"
    "v_mfma_f32_16x16x32_bf16 v[100:103], a[196:199], a[148:151], v[100:103]\n"
    "v_mfma_f32_16x16x32_bf16 v[100:103], a[200:203], a[152:155], v[100:103]\n"
    "v_mfma_f32_16x16x32_bf16 v[100:103], a[204:207], a[156:159], v[100:103]\n"
    "buffer_load_dwordx4 a[228:231], v0, s[56:59], s72 offen offset:1024 sc1\n"
    "v_mfma_f32_16x16x32_bf16 v[104:107], a[192:195], a[160:163], v[32:35]\n"
    "v_exp_f32 v7, v81\n"
    "v_mfma_f32_16x16x32_bf16 v[104:107], a[196:199], a[164:167], v[104:107]\n"
    "v_exp_f32 v2, v82\n"
    "v_mfma_f32_16x16x32_bf16 v[104:107], a[200:203], a[168:171], v[104:107]\n"
    "v_exp_f32 v3, v83\n"
    "v_mfma_f32_16x16x32_bf16 v[104:107], a[204:207], a[172:175], v[104:107]\n"
    "buffer_load_dwordx4 a[232:235], v0, s[56:59], s72 offen offset:2048 sc1\n"
    "v_cvt_pk_bf16_f32 v130, v6, v7\n"
    "v_mfma_f32_16x16x32_bf16 v[108:111], a[192:195], a[176:179], v[32:35]\n"
    "v_cvt_pk_bf16_f32 v131, v2, v3\n"
    "v_mfma_f32_16x16x32_bf16 v[108:111], a[196:199], a[180:183], v[108:111]\n"
    "v_exp_f32 v4, v68\n"
    "v_mfma_f32_16x16x32_bf16 v[108:111], a[200:203], a[184:187], v[108:111]\n"
    "v_mfma_f32_16x16x32_bf16 v[108:111], a[204:207], a[188:191], v[108:111]\n"
    "buffer_load_dwordx4 a[236:239], v0, s[56:59], s72 offen offset:3072 sc1\n"
    "v_mfma_f32_16x16x32_bf16 v[112:115], a[208:211], a[128:131], v[36:39]\n"
    "v_exp_f32 v5, v69\n"
    "v_mfma_f32_16x16x32_bf16 v[112:115], a[212:215], a[132:135], v[112:115]\n"
    "v_exp_f32 v6, v70\n"
    "v_mfma_f32_16x16x32_bf16 v[112:115], a[216:219], a[136:139], v[112:115]\n"
    "v_exp_f32 v7, v71\n"
    "v_mfma_f32_16x16x32_bf16 v[112:115], a[220:223], a[140:143], v[112:115]\n"
    "buffer_load_dwordx4 a[240:243], v1, s[56:59], s72 offen offset:0 sc1\n"
    "v_cvt_pk_bf16_f32 v132, v4, v5\n"
    "v_mfma_f32_16x16x32_bf16 v[116:119], a[208:211], a[144:147], v[36:39]\n"
    "v_cvt_pk_bf16_f32 v133, v6, v7\n"
    "v_mfma_f32_16x16x32_bf16 v[116:119], a[212:215], a[148:151], v[116:119]\n"
    "v_exp_f32 v2, v84\n"
    "v_mfma_f32_16x16x32_bf16 v[116:119], a[216:219], a[152:155], v[116:119]\n"
    "v_mfma_f32_16x16x32_bf16 v[116:119], a[220:223], a[156:159], v[116:119]\n"
    "buffer_load_dwordx4 a[244:247], v1, s[56:59], s72 offen offset:1024 sc1\n"
    "v_mfma_f32_16x16x32_bf16 v[120:123], a[208:211], a[160:163], v[36:39]\n"
    "v_exp_f32 v3, v85\n"
    "v_mfma_f32_16x16x32_bf16 v[120:123], a[212:215], a[164:167], v[120:123]\n"
    "v_exp_f32 v4, v86\n"
    "v_mfma_f32_16x16x32_bf16 v[120:123], a[216:219], a[168:171], v[120:123]\n"
    "v_exp_f32 v5, v87\n"
    "v_mfma_f32_16x16x32_bf16 v[120:123], a[220:223], a[172:175], v[120:123]\n"
    "buffer_load_dwordx4 a[248:251], v1, s[56:59], s72 offen offset:2048 sc1\n"
    "v_cvt_pk_bf16_f32 v134, v2, v3\n"
    "v_mfma_f32_16x16x32_bf16 v[124:127], a[208:211], a[176:179], v[36:39]\n"
    "v_cvt_pk_bf16_f32 v135, v4, v5\n"
    "v_mfma_f32_16x16x32_bf16 v[124:127], a[212:215], a[180:183], v[124:127]\n"
    "v_exp_f32 v6, v72\n"
    "v_mfma_f32_16x16x32_bf16 v[124:127], a[216:219], a[184:187], v[124:127]\n"
    "v_exp_f32 v7, v73\n"
    "v_mfma_f32_16x16x32_bf16 v[124:127], a[220:223], a[188:191], v[124:127]\n"
    "buffer_load_dwordx4 a[252:255], v1, s[56:59], s72 offen offset:3072 sc1\n"
    "s_waitcnt vmcnt(12)\n"
    "v_mfma_f32_16x16x32_bf16 a[0:3], v[144:147], v[192:195], a[0:3]\n"
    "v_exp_f32 v2, v74\n"
    "v_mfma_f32_16x16x32_bf16 a[4:7], v[148:151], v[192:195], a[4:7]\n"
    "v_cvt_pk_bf16_f32 v136, v6, v7\n"
    "v_mfma_f32_16x16x32_bf16 a[8:11], v[152:155], v[192:195], a[8:11]\n"
    "v_exp_f32 v3, v75\n"
    "v_mfma_f32_16x16x32_bf16 v[160:163], v[156:159], v[192:195], v[160:163]\n"
    "buffer_load_dwordx4 a[12:15], v0, s[8:11], s72 offen offset:0 sc1\n"
    "v_mfma_f32_16x16x32_bf16 a[16:19], v[144:147], v[196:199], a[16:19]\n"
    "v_exp_f32 v4, v88\n"
    "v_mfma_f32_16x16x32_bf16 a[20:23], v[148:151], v[196:199], a[20:23]\n"
    "ds_read_b128 v[32:35], v9 offset:1536\n"
    "v_mfma_f32_16x16x32_bf16 a[24:27], v[152:155], v[196:199], a[24:27]\n"
    "v_exp_f32 v5, v89\n"
    "v_mfma_f32_16x16x32_bf16 v[164:167], v[156:159], v[196:199], v[164:167]\n"
    "buffer_load_dwordx4 a[28:31], v0, s[8:11], s72 offen offset:1024 sc1\n"
    "v_mfma_f32_16x16x32_bf16 a[32:35], v[144:147], v[200:203], a[32:35]\n"
    "v_cvt_pk_bf16_f32 v137, v2, v3\n"
    "v_cvt_pk_bf16_f32 v138, v4, v5\n"
    "v_mfma_f32_16x16x32_bf16 a[36:39], v[148:151], v[200:203], a[36:39]\n"
    "ds_read_b128 v[36:39], v9 offset:1552\n"
    "v_mfma_f32_16x16x32_bf16 a[40:43], v[152:155], v[200:203], a[40:43]\n"
    "v_exp_f32 v6, v90\n"
    "v_mfma_f32_16x16x32_bf16 v[168:171], v[156:159], v[200:203], v[168:171]\n"
    "buffer_load_dwordx4 a[44:47], v0, s[8:11], s72 offen offset:2048 sc1\n"
    "v_mfma_f32_16x16x32_bf16 a[48:51], v[144:147], v[204:207], a[48:51]\n"
    "v_exp_f32 v7, v91\n"
    "v_mfma_f32_16x16x32_bf16 a[52:55], v[148:151], v[204:207], a[52:55]\n"
    "ds_read_b128 v[60:63], v9 offset:1184\n"
    "v_mfma_f32_16x16x32_bf16 a[56:59], v[152:155], v[204:207], a[56:59]\n"
    "v_exp_f32 v2, v76\n"
    "v_mfma_f32_16x16x32_bf16 v[172:175], v[156:159], v[204:207], v[172:175]\n"
    "buffer_load_dwordx4 a[60:63], v0, s[8:11], s72 offen offset:3072 sc1\n"
    "s_waitcnt vmcnt(12)\n"
    "v_mfma_f32_16x16x32_bf16 a[64:67], v[144:147], v[208:211], a[64:67]\n"
    "v_exp_f32 v3, v77\n"
    "v_mfma_f32_16x16x32_bf16 a[68:71], v[148:151], v[208:211], a[68:71]\n"
    "v_cvt_pk_bf16_f32 v139, v6, v7\n"
    "v_mfma_f32_16x16x32_bf16 a[72:75], v[152:155], v[208:211], a[72:75]\n"
    "v_exp_f32 v4, v78\n"
    "v_mfma_f32_16x16x32_bf16 v[176:179], v[156:159], v[208:211], v[176:179]\n"
    "buffer_load_dwordx4 a[76:79], v1, s[8:11], s72 offen offset:0 sc1\n"
    "v_mfma_f32_16x16x32_bf16 a[80:83], v[144:147], v[212:215], a[80:83]\n"
    "v_exp_f32 v5, v79\n"
    "v_mfma_f32_16x16x32_bf16 a[84:87], v[148:151], v[212:215], a[84:87]\n"
    "v_cvt_pk_bf16_f32 v140, v2, v3\n"
    "v_mfma_f32_16x16x32_bf16 a[88:91], v[152:155], v[212:215], a[88:91]\n"
    "v_exp_f32 v6, v92\n"
    "v_mfma_f32_16x16x32_bf16 v[180:183], v[156:159], v[212:215], v[180:183]\n"
    "buffer_load_dwordx4 a[92:95], v1, s[8:11], s72 offen offset:1024 sc1\n"
    "v_mfma_f32_16x16x32_bf16 a[96:99], v[144:147], v[216:219], a[96:99]\n"
    "v_exp_f32 v7, v93\n"
    "v_mfma_f32_16x16x32_bf16 a[100:103], v[148:151], v[216:219], a[100:103]\n"
    "v_cvt_pk_bf16_f32 v141, v4, v5\n"
    "v_mfma_f32_16x16x32_bf16 a[104:107], v[152:155], v[216:219], a[104:107]\n"
    "v_exp_f32 v2, v94\n"
    "v_mfma_f32_16x16x32_bf16 v[184:187], v[156:159], v[216:219], v[184:187]\n"
    "buffer_load_dwordx4 a[108:111], v1, s[8:11], s72 offen offset:2048 sc1\n"
    "v_mfma_f32_16x16x32_bf16 a[112:115], v[144:147], v[220:223], a[112:115]\n"
    "v_exp_f32 v3, v95\n"
    "v_mfma_f32_16x16x32_bf16 a[116:119], v[148:151], v[220:223], a[116:119]\n"
    "v_cvt_pk_bf16_f32 v142, v6, v7\n"
    "v_mfma_f32_16x16x32_bf16 a[120:123], v[152:155], v[220:223], a[120:123]\n"
    "v_cvt_pk_bf16_f32 v143, v2, v3\n"
    "v_mfma_f32_16x16x32_bf16 v[188:191], v[156:159], v[220:223], v[188:191]\n"
    "buffer_load_dwordx4 a[124:127], v1, s[8:11], s72 offen offset:3072 sc1\n"
    "s_waitcnt lgkmcnt(0)\n"
    "s_cbranch_vccnz .Lf_STUB7\n"
    ".Lf_BACK7:\n"
    "s_or_b32 s88, %[bid], %[w]\n"
    "s_cbranch_scc1 .Lf_SKg\n"
    "v_mul_u32_u24 v240, 128, v25\n"
    "buffer_load_dwordx2 v[224:225], v240, s[56:59], s69 offen sc1\n"
    "v_add_u32 v240, 8192, v240\n"
    "buffer_load_dwordx2 v[226:227], v240, s[56:59], s69 offen sc1\n"
    "v_add_u32 v240, 8192, v240\n"
    "buffer_load_dwordx2 v[228:229], v240, s[56:59], s69 offen sc1\n"
    "v_add_u32 v240, 8192, v240\n"
    "buffer_load_dwordx2 v[230:231], v240, s[56:59], s69 offen sc1\n"
    "v_add_u32 v240, 8192, v240\n"
    ".Lf_SKg:\n"
    "v_mfma_f32_16x16x32_bf16 v[10:13], v[60:63], v[128:131], v[10:13]\n"
    "v_exp_f32 v2, v96\n"
    "v_mfma_f32_16x16x32_bf16 v[14:17], v[60:63], v[132:135], v[14:17]\n"
    "v_exp_f32 v3, v97\n"
    "v_mfma_f32_16x16x32_bf16 v[40:43], v[60:63], v[136:139], v[40:43]\n"
    "v_exp_f32 v4, v98\n"
    "v_mfma_f32_16x16x32_bf16 v[44:47], v[60:63], v[140:143], v[44:47]\n"
    "v_cvt_pk_bf16_f32 v144, v2, v3\n"
    "s_waitcnt vmcnt(12)\n"
    "v_mfma_f32_16x16x32_bf16 v[64:67], a[224:227], a[128:131], v[32:35]\n"
    "v_exp_f32 v5, v99\n"
    "v_mfma_f32_16x16x32_bf16 v[64:67], a[228:231], a[132:135], v[64:67]\n"
    "v_cvt_pk_bf16_f32 v145, v4, v5\n"
    "v_mfma_f32_16x16x32_bf16 v[64:67], a[232:235], a[136:139], v[64:67]\n"
    "v_exp_f32 v6, v112\n"
    "v_mfma_f32_16x16x32_bf16 v[64:67], a[236:239], a[140:143], v[64:67]\n"
    "s_add_u32 s72, s72, 0x8000\n"
    "buffer_load_dwordx4 a[192:195], v0, s[56:59], s72 offen offset:0 sc1\n"
    "v_mfma_f32_16x16x32_bf16 v[68:71], a[224:227], a[144:147], v[32:35]\n"
    "v_mfma_f32_16x16x32_bf16 v[68:71], a[228:231], a[148:151], v[68:71]\n"
    "v_exp_f32 v7, v113\n"
    "v_mfma_f32_16x16x32_bf16 v[68:71], a[232:235], a[152:155], v[68:71]\n"
    "v_exp_f32 v2, v114\n"
    "v_mfma_f32_16x16x32_bf16 v[68:71], a[236:239], a[156:159], v[68:71]\n"
    "buffer_load_dwordx4 a[196:199], v0, s[56:59], s72 offen offset:1024 sc1\n"
    "v_mfma_f32_16x16x32_bf16 v[72:75], a[224:227], a[160:163], v[32:35]\n"
    "v_exp_f32 v3, v115\n"
    "v_mfma_f32_16x16x32_bf16 v[72:75], a[228:231], a[164:167], v[72:75]\n"
    "v_cvt_pk_bf16_f32 v146, v6, v7\n"
    "v_mfma_f32_16x16x32_bf16 v[72:75], a[232:235], a[168:171], v[72:75]\n"
    "v_exp_f32 v4, v100\n"
    "v_mfma_f32_16x16x32_bf16 v[72:75], a[236:239], a[172:175], v[72:75]\n"
    "buffer_load_dwordx4 a[200:203], v0, s[56:59], s72 offen offset:2048 sc1\n"
    "v_mfma_f32_16x16x32_bf16 v[76:79], a[224:227], a[176:179], v[32:35]\n"
    "v_exp_f32 v5, v101\n"
    "v_mfma_f32_16x16x32_bf16 v[76:79], a[228:231], a[180:183], v[76:79]\n"
    "v_cvt_pk_bf16_f32 v147, v2, v3\n"
    "v_mfma_f32_16x16x32_bf16 v[76:79], a[232:235], a[184:187], v[76:79]\n"
    "v_cvt_pk_bf16_f32 v148, v4, v5\n"
    "v_mfma_f32_16x16x32_bf16 v[76:79], a[236:239], a[188:191], v[76:79]\n"
    "buffer_load_dwordx4 a[204:207], v0, s[56:59], s72 offen offset:3072 sc1\n"
    "s_waitcnt vmcnt(12)\n"
    "v_mfma_f32_16x16x32_bf16 v[80:83], a[240:243], a[128:131], v[36:39]\n"
    "v_exp_f32 v6, v102\n"
    "v_mfma_f32_16x16x32_bf16 v[80:83], a[244:247], a[132:135], v[80:83]\n"
    "v_exp_f32 v7, v103\n"
    "v_mfma_f32_16x16x32_bf16 v[80:83], a[248:251], a[136:139], v[80:83]\n"
    "v_cvt_pk_bf16_f32 v149, v6, v7\n"
    "v_mfma_f32_16x16x32_bf16 v[80:83], a[252:255], a[140:143], v[80:83]\n"
    "buffer_load_dwordx4 a[208:211], v1, s[56:59], s72 offen offset:0 sc1\n"
    "v_mfma_f32_16x16x32_bf16 v[84:87], a[240:243], a[144:147], v[36:39]\n"
    "v_exp_f32 v2, v116\n"
    "v_mfma_f32_16x16x32_bf16 v[84:87], a[244:247], a[148:151], v[84:87]\n"
    "v_exp_f32 v3, v117\n"
    "v_mfma_f32_16x16x32_bf16 v[84:87], a[248:251], a[152:155], v[84:87]\n"
    "v_cvt_pk_bf16_f32 v150, v2, v3\n"
    "v_mfma_f32_16x16x32_bf16 v[84:87], a[252:255], a[156:159], v[84:87]\n"
    "buffer_load_dwordx4 a[212:215], v1, s[56:59], s72 offen offset:1024 sc1\n"
    "v_mfma_f32_16x16x32_bf16 v[88:91], a[240:243], a[160:163], v[36:39]\n"
    "v_exp_f32 v4, v118\n"
    "v_mfma_f32_16x16x32_bf16 v[88:91], a[244:247], a[164:167], v[88:91]\n"
    "v_exp_f32 v5, v119\n"
    "v_mfma_f32_16x16x32_bf16 v[88:91], a[248:251], a[168:171], v[88:91]\n"
    "v_cvt_pk_bf16_f32 v151, v4, v5\n"
    "v_mfma_f32_16x16x32_bf16 v[88:91], a[252:255], a[172:175], v[88:91]\n"
    "buffer_load_dwordx4 a[216:219], v1, s[56:59], s72 offen offset:2048 sc1\n"
    "v_mfma_f32_16x16x32_bf16 v[92:95], a[240:243], a[176:179], v[36:39]\n"
    "v_exp_f32 v6, v104\n"
    "v_mfma_f32_16x16x32_bf16 v[92:95], a[244:247], a[180:183], v[92:95]\n"
    "v_exp_f32 v7, v105\n"
    "v_mfma_f32_16x16x32_bf16 v[92:95], a[248:251], a[184:187], v[92:95]\n"
    "v_cvt_pk_bf16_f32 v152, v6, v7\n"
    "v_mfma_f32_16x16x32_bf16 v[92:95], a[252:255], a[188:191], v[92:95]\n"
    "buffer_load_dwordx4 a[220:223], v1, s[56:59], s72 offen offset:3072 sc1\n"
    "s_waitcnt vmcnt(12)\n"
    "v_mfma_f32_16x16x32_bf16 a[0:3], v[128:131], a[12:15], a[0:3]\n"
    "v_exp_f32 v2, v106\n"
    "v_mfma_f32_16x16x32_bf16 a[4:7], v[132:135], a[12:15], a[4:7]\n"
    "v_mfma_f32_16x16x32_bf16 a[8:11], v[136:139], a[12:15], a[8:11]\n"
    "v_exp_f32 v3, v107\n"
    "v_mfma_f32_16x16x32_bf16 v[160:163], v[140:143], a[12:15], v[160:163]\n"
    "buffer_load_dwordx4 v[192:195], v0, s[8:11], s72 offen offset:0 sc1\n"
    "v_cvt_pk_bf16_f32 v153, v2, v3\n"
    "v_mfma_f32_16x16x32_bf16 a[16:19], v[128:131], a[28:31], a[16:19]\n"
    "v_exp_f32 v4, v120\n"
    "v_mfma_f32_16x16x32_bf16 a[20:23], v[132:135], a[28:31], a[20:23]\n"
    "ds_read_b128 v[32:35], v9 offset:1728\n"
    "v_mfma_f32_16x16x32_bf16 a[24:27], v[136:139], a[28:31], a[24:27]\n"
    "v_exp_f32 v5, v121\n"
    "v_mfma_f32_16x16x32_bf16 v[164:167], v[140:143], a[28:31], v[164:167]\n"
    "buffer_load_dwordx4 v[196:199], v0, s[8:11], s72 offen offset:1024 sc1\n"
    "v_mfma_f32_16x16x32_bf16 a[32:35], v[128:131], a[44:47], a[32:35]\n"
    "v_exp_f32 v6, v122\n"
    "v_mfma_f32_16x16x32_bf16 a[36:39], v[132:135], a[44:47], a[36:39]\n"
    "ds_read_b128 v[36:39], v9 offset:1744\n"
    "v_mfma_f32_16x16x32_bf16 a[40:43], v[136:139], a[44:47], a[40:43]\n"
    "v_exp_f32 v7, v123\n"
    "v_mfma_f32_16x16x32_bf16 v[168:171], v[140:143], a[44:47], v[168:171]\n"
    "buffer_load_dwordx4 v[200:203], v0, s[8:11], s72 offen offset:2048 sc1\n"
    "v_mfma_f32_16x16x32_bf16 a[48:51], v[128:131], a[60:63], a[48:51]\n"
    "v_cvt_pk_bf16_f32 v154, v4, v5\n"
    "v_cvt_pk_bf16_f32 v155, v6, v7\n"
    "v_mfma_f32_16x16x32_bf16 a[52:55], v[132:135], a[60:63], a[52:55]\n"
    "ds_read_b128 v[60:63], v9 offset:1376\n"
    "v_mfma_f32_16x16x32_bf16 a[56:59], v[136:139], a[60:63], a[56:59]\n"
    "v_exp_f32 v2, v108\n"
    "v_mfma_f32_16x16x32_bf16 v[172:175], v[140:143], a[60:63], v[172:175]\n"
    "buffer_load_dwordx4 v[204:207], v0, s[8:11], s72 offen offset:3072 sc1\n"
    "s_waitcnt vmcnt(12)\n"
    "v_mfma_f32_16x16x32_bf16 a[64:67], v[128:131], a[76:79], a[64:67]\n"
    "v_mfma_f32_16x16x32_bf16 a[68:71], v[132:135], a[76:79], a[68:71]\n"
    "v_exp_f32 v3, v109\n"
    "v_mfma_f32_16x16x32_bf16 a[72:75], v[136:139], a[76:79], a[72:75]\n"
    "v_exp_f32 v4, v110\n"
    "v_mfma_f32_16x16x32_bf16 v[176:179], v[140:143], a[76:79], v[176:179]\n"
    "buffer_load_dwordx4 v[208:211], v1, s[8:11], s72 offen offset:0 sc1\n"
    "v_cvt_pk_bf16_f32 v156, v2, v3\n"
    "v_mfma_f32_16x16x32_bf16 a[80:83], v[128:131], a[92:95], a[80:83]\n"
    "v_mfma_f32_16x16x32_bf16 a[84:87], v[132:135], a[92:95], a[84:87]\n"
    "v_exp_f32 v5, v111\n"
    "v_mfma_f32_16x16x32_bf16 a[88:91], v[136:139], a[92:95], a[88:91]\n"
    "v_exp_f32 v6, v124\n"
    "v_mfma_f32_16x16x32_bf16 v[180:183], v[140:143], a[92:95], v[180:183]\n"
    "buffer_load_dwordx4 v[212:215], v1, s[8:11], s72 offen offset:1024 sc1\n"
    "v_cvt_pk_bf16_f32 v157, v4, v5\n"
    "v_mfma_f32_16x16x32_bf16 a[96:99], v[128:131], a[108:111], a[96:99]\n"
    "v_mfma_f32_16x16x32_bf16 a[100:103], v[132:135], a[108:111], a[100:103]\n"
    "v_exp_f32 v7, v125\n"
    "v_mfma_f32_16x16x32_bf16 a[104:107], v[136:139], a[108:111], a[104:107]\n"
    "v_exp_f32 v2, v126\n"
    "v_mfma_f32_16x16x32_bf16 v[184:187], v[140:143], a[108:111], v[184:187]\n"
    "buffer_load_dwordx4 v[216:219], v1, s[8:11], s72 offen offset:2048 sc1\n"
    "v_cvt_pk_bf16_f32 v158, v6, v7\n"
    "v_mfma_f32_16x16x32_bf16 a[112:115], v[128:131], a[124:127], a[112:115]\n"
    "v_mfma_f32_16x16x32_bf16 a[116:119], v[132:135], a[124:127], a[116:119]\n"
    "v_exp_f32 v3, v127\n"
    "v_mfma_f32_16x16x32_bf16 a[120:123], v[136:139], a[124:127], a[120:123]\n"
    "v_cvt_pk_bf16_f32 v159, v2, v3\n"
    "v_mfma_f32_16x16x32_bf16 v[188:191], v[140:143], a[124:127], v[188:191]\n"
    "buffer_load_dwordx4 v[220:223], v1, s[8:11], s72 offen offset:3072 sc1\n"
    "s_waitcnt lgkmcnt(0)\n"
    "s_cbranch_vccnz .Lf_STUB8\n"
    ".Lf_BACK8:\n"
    "v_mfma_f32_16x16x32_bf16 v[10:13], v[60:63], v[144:147], v[10:13]\n"
    "v_exp_f32 v2, v64\n"
    "v_mfma_f32_16x16x32_bf16 v[14:17], v[60:63], v[148:151], v[14:17]\n"
    "v_exp_f32 v3, v65\n"
    "v_mfma_f32_16x16x32_bf16 v[40:43], v[60:63], v[152:155], v[40:43]\n"
    "v_exp_f32 v4, v66\n"
    "v_mfma_f32_16x16x32_bf16 v[44:47], v[60:63], v[156:159], v[44:47]\n"
    "v_cvt_pk_bf16_f32 v128, v2, v3\n"
    "s_waitcnt vmcnt(12)\n"
    "v_mfma_f32_16x16x32_bf16 v[96:99], a[192:195], a[128:131], v[32:35]\n"
    "v_exp_f32 v5, v67\n"
    "v_mfma_f32_16x16x32_bf16 v[96:99], a[196:199], a[132:135], v[96:99]\n"
    "v_cvt_pk_bf16_f32 v129, v4, v5\n"
    "v_mfma_f32_16x16x32_bf16 v[96:99], a[200:203], a[136:139], v[96:99]\n"
    "v_exp_f32 v6, v80\n"
    "v_mfma_f32_16x16x32_bf16 v[96:99], a[204:207], a[140:143], v[96:99]\n"
    "s_add_u32 s72, s72, 0x8000\n"
    "buffer_load_dwordx4 a[224:227], v0, s[56:59], s72 offen offset:0 sc1\n"
    "v_mfma_f32_16x16x32_bf16 v[100:103], a[192:195], a[144:147], v[32:35]\n"
    "v_mfma_f32_16x16x32_bf16 v[100:103], a[196:199], a[148:151], v[100:103]\n"
    "v_exp_f32 v7, v81\n"
    "v_mfma_f32_16x16x32_bf16 v[100:103], a[200:203], a[152:155], v[100:103]\n"
    "v_exp_f32 v2, v82\n"
    "v_mfma_f32_16x16x32_bf16 v[100:103], a[204:207], a[156:159], v[100:103]\n"
    "buffer_load_dwordx4 a[228:231], v0, s[56:59], s72 offen offset:1024 sc1\n"
    "v_mfma_f32_16x16x32_bf16 v[104:107], a[192:195], a[160:163], v[32:35]\n"
    "v_exp_f32 v3, v83\n"
    "v_mfma_f32_16x16x32_bf16 v[104:107], a[196:199], a[164:167], v[104:107]\n"
    "v_cvt_pk_bf16_f32 v130, v6, v7\n"
    "v_mfma_f32_16x16x32_bf16 v[104:107], a[200:203], a[168:171], v[104:107]\n"
    "v_exp_f32 v4, v68\n"
    "v_mfma_f32_16x16x32_bf16 v[104:107], a[204:207], a[172:175], v[104:107]\n"
    "buffer_load_dwordx4 a[232:235], v0, s[56:59], s72 offen offset:2048 sc1\n"
    "v_mfma_f32_16x16x32_bf16 v[108:111], a[192:195], a[176:179], v[32:35]\n"
    "v_exp_f32 v5, v69\n"
    "v_mfma_f32_16x16x32_bf16 v[108:111], a[196:199], a[180:183], v[108:111]\n"
    "v_cvt_pk_bf16_f32 v131, v2, v3\n"
    "v_mfma_f32_16x16x32_bf16 v[108:111], a[200:203], a[184:187], v[108:111]\n"
    "v_cvt_pk_bf16_f32 v132, v4, v5\n"
    "v_mfma_f32_16x16x32_bf16 v[108:111], a[204:207], a[188:191], v[108:111]\n"
    "buffer_load_dwordx4 a[236:239], v0, s[56:59], s72 offen offset:3072 sc1\n"
    "s_waitcnt vmcnt(12)\n"
    "v_mfma_f32_16x16x32_bf16 v[112:115], a[208:211], a[128:131], v[36:39]\n"
    "v_exp_f32 v6, v70\n"
    "v_mfma_f32_16x16x32_bf16 v[112:115], a[212:215], a[132:135], v[112:115]\n"
    "v_exp_f32 v7, v71\n"
    "v_mfma_f32_16x16x32_bf16 v[112:115], a[216:219], a[136:139], v[112:115]\n"
    "v_cvt_pk_bf16_f32 v133, v6, v7\n"
    "v_mfma_f32_16x16x32_bf16 v[112:115], a[220:223], a[140:143], v[112:115]\n"
    "buffer_load_dwordx4 a[240:243], v1, s[56:59], s72 offen offset:0 sc1\n"
    "v_mfma_f32_16x16x32_bf16 v[116:119], a[208:211], a[144:147], v[36:39]\n"
    "v_exp_f32 v2, v84\n"
    "v_mfma_f32_16x16x32_bf16 v[116:119], a[212:215], a[148:151], v[116:119]\n"
    "v_exp_f32 v3, v85\n"
    "v_mfma_f32_16x16x32_bf16 v[116:119], a[216:219], a[152:155], v[116:119]\n"
    "v_cvt_pk_bf16_f32 v134, v2, v3\n"
    "v_mfma_f32_16x16x32_bf16 v[116:119], a[220:223], a[156:159], v[116:119]\n"
    "buffer_load_dwordx4 a[244:247], v1, s[56:59], s72 offen offset:1024 sc1\n"
    "v_mfma_f32_16x16x32_bf16 v[120:123], a[208:211], a[160:163], v[36:39]\n"
    "v_exp_f32 v4, v86\n"
    "v_mfma_f32_16x16x32_bf16 v[120:123], a[212:215], a[164:167], v[120:123]\n"
    "v_exp_f32 v5, v87\n"
    "v_mfma_f32_16x16x32_bf16 v[120:123], a[216:219], a[168:171], v[120:123]\n"
    "v_cvt_pk_bf16_f32 v135, v4, v5\n"
    "v_mfma_f32_16x16x32_bf16 v[120:123], a[220:223], a[172:175], v[120:123]\n"
    "buffer_load_dwordx4 a[248:251], v1, s[56:59], s72 offen offset:2048 sc1\n"
    "v_mfma_f32_16x16x32_bf16 v[124:127], a[208:211], a[176:179], v[36:39]\n"
    "v_exp_f32 v6, v72\n"
    "v_mfma_f32_16x16x32_bf16 v[124:127], a[212:215], a[180:183], v[124:127]\n"
    "v_exp_f32 v7, v73\n"
    "v_mfma_f32_16x16x32_bf16 v[124:127], a[216:219], a[184:187], v[124:127]\n"
    "v_cvt_pk_bf16_f32 v136, v6, v7\n"
    "v_mfma_f32_16x16x32_bf16 v[124:127], a[220:223], a[188:191], v[124:127]\n"
    "buffer_load_dwordx4 a[252:255], v1, s[56:59], s72 offen offset:3072 sc1\n"
    "s_waitcnt vmcnt(12)\n"
    "v_mfma_f32_16x16x32_bf16 a[0:3], v[144:147], v[192:195], a[0:3]\n"
    "v_exp_f32 v2, v74\n"
    "v_mfma_f32_16x16x32_bf16 a[4:7], v[148:151], v[192:195], a[4:7]\n"
    "v_mfma_f32_16x16x32_bf16 a[8:11], v[152:155], v[192:195], a[8:11]\n"
    "v_exp_f32 v3, v75\n"
    "v_mfma_f32_16x16x32_bf16 v[160:163], v[156:159], v[192:195], v[160:163]\n"
    "buffer_load_dwordx4 a[12:15], v0, s[8:11], s72 offen offset:0 sc1\n"
    "v_cvt_pk_bf16_f32 v137, v2, v3\n"
    "v_mfma_f32_16x16x32_bf16 a[16:19], v[144:147], v[196:199], a[16:19]\n"
    "v_exp_f32 v4, v88\n"
    "v_mfma_f32_16x16x32_bf16 a[20:23], v[148:151], v[196:199], a[20:23]\n"
    "ds_read_b128 v[32:35], v9 offset:1920\n"
    "v_mfma_f32_16x16x32_bf16 a[24:27], v[152:155], v[196:199], a[24:27]\n"
    "v_exp_f32 v5, v89\n"
    "v_mfma_f32_16x16x32_bf16 v[164:167], v[156:159], v[196:199], v[164:167]\n"
    "buffer_load_dwordx4 a[28:31], v0, s[8:11], s72 offen offset:1024 sc1\n"
    "v_mfma_f32_16x16x32_bf16 a[32:35], v[144:147], v[200:203], a[32:35]\n"
    "v_exp_f32 v6, v90\n"
    "v_mfma_f32_16x16x32_bf16 a[36:39], v[148:151], v[200:203], a[36:39]\n"
    "ds_read_b128 v[36:39], v9 offset:1936\n"
    "v_mfma_f32_16x16x32_bf16 a[40:43], v[152:155], v[200:203], a[40:43]\n"
    "v_exp_f32 v7, v91\n"
    "v_mfma_f32_16x16x32_bf16 v[168:171], v[156:159], v[200:203], v[168:171]\n"
    "buffer_load_dwordx4 a[44:47], v0, s[8:11], s72 offen offset:2048 sc1\n"
    "v_mfma_f32_16x16x32_bf16 a[48:51], v[144:147], v[204:207], a[48:51]\n"
    "v_cvt_pk_bf16_f32 v138, v4, v5\n"
    "v_cvt_pk_bf16_f32 v139, v6, v7\n"
    "v_mfma_f32_16x16x32_bf16 a[52:55], v[148:151], v[204:207], a[52:55]\n"
    "ds_read_b128 v[60:63], v9 offset:1568\n"
    "v_mfma_f32_16x16x32_bf16 a[56:59], v[152:155], v[204:207], a[56:59]\n"
    "v_exp_f32 v2, v76\n"
    "v_mfma_f32_16x16x32_bf16 v[172:175], v[156:159], v[204:207], v[172:175]\n"
    "buffer_load_dwordx4 a[60:63], v0, s[8:11], s72 offen offset:3072 sc1\n"
    "s_waitcnt vmcnt(12)\n"
    "v_mfma_f32_16x16x32_bf16 a[64:67], v[144:147], v[208:211], a[64:67]\n"
    "v_mfma_f32_16x16x32_bf16 a[68:71], v[148:151], v[208:211], a[68:71]\n"
    "v_exp_f32 v3, v77\n"
    "v_mfma_f32_16x16x32_bf16 a[72:75], v[152:155], v[208:211], a[72:75]\n"
    "v_exp_f32 v4, v78\n"
    "v_mfma_f32_16x16x32_bf16 v[176:179], v[156:159], v[208:211], v[176:179]\n"
    "buffer_load_dwordx4 a[76:79], v1, s[8:11], s72 offen offset:0 sc1\n"
    "v_cvt_pk_bf16_f32 v140, v2, v3\n"
    "v_mfma_f32_16x16x32_bf16 a[80:83], v[144:147], v[212:215], a[80:83]\n"
    "v_mfma_f32_16x16x32_bf16 a[84:87], v[148:151], v[212:215], a[84:87]\n"
    "v_exp_f32 v5, v79\n"
    "v_mfma_f32_16x16x32_bf16 a[88:91], v[152:155], v[212:215], a[88:91]\n"
    "v_exp_f32 v6, v92\n"
    "v_mfma_f32_16x16x32_bf16 v[180:183], v[156:159], v[212:215], v[180:183]\n"
    "buffer_load_dwordx4 a[92:95], v1, s[8:11], s72 offen offset:1024 sc1\n"
    "v_cvt_pk_bf16_f32 v141, v4, v5\n"
    "v_mfma_f32_16x16x32_bf16 a[96:99], v[144:147], v[216:219], a[96:99]\n"
    "v_mfma_f32_16x16x32_bf16 a[100:103], v[148:151], v[216:219], a[100:103]\n"
    "v_exp_f32 v7, v93\n"
    "v_mfma_f32_16x16x32_bf16 a[104:107], v[152:155], v[216:219], a[104:107]\n"
    "v_exp_f32 v2, v94\n"
    "v_mfma_f32_16x16x32_bf16 v[184:187], v[156:159], v[216:219], v[184:187]\n"
    "buffer_load_dwordx4 a[108:111], v1, s[8:11], s72 offen offset:2048 sc1\n"
    "v_cvt_pk_bf16_f32 v142, v6, v7\n"
    "v_mfma_f32_16x16x32_bf16 a[112:115], v[144:147], v[220:223], a[112:115]\n"
    "v_mfma_f32_16x16x32_bf16 a[116:119], v[148:151], v[220:223], a[116:119]\n"
    "v_exp_f32 v3, v95\n"
    "v_mfma_f32_16x16x32_bf16 a[120:123], v[152:155], v[220:223], a[120:123]\n"
    "v_cvt_pk_bf16_f32 v143, v2, v3\n"
    "v_mfma_f32_16x16x32_bf16 v[188:191], v[156:159], v[220:223], v[188:191]\n"
    "buffer_load_dwordx4 a[124:127], v1, s[8:11], s72 offen offset:3072 sc1\n"
    "s_waitcnt lgkmcnt(0)\n"
    "s_cbranch_vccnz .Lf_STUB9\n"
    ".Lf_BACK9:\n"
    "s_or_b32 s88, %[bid], %[w]\n"
    "s_cbranch_scc1 .Lf_SKh\n"
    "s_call_b64 s[76:77], .Lf_EUPD\n"
    ".Lf_SKh:\n"
    "v_mfma_f32_16x16x32_bf16 v[10:13], v[60:63], v[128:131], v[10:13]\n"
    "v_exp_f32 v2, v96\n"
    "v_mfma_f32_16x16x32_bf16 v[14:17], v[60:63], v[132:135], v[14:17]\n"
    "v_exp_f32 v3, v97\n"
    "v_mfma_f32_16x16x32_bf16 v[40:43], v[60:63], v[136:139], v[40:43]\n"
    "v_exp_f32 v4, v98\n"
    "v_mfma_f32_16x16x32_bf16 v[44:47], v[60:63], v[140:143], v[44:47]\n"
    "v_exp_f32 v5, v99\n"
    "s_waitcnt vmcnt(12)\n"
    "v_mfma_f32_16x16x32_bf16 v[64:67], a[224:227], a[128:131], v[32:35]\n"
    "v_cvt_pk_bf16_f32 v144, v2, v3\n"
    "v_mfma_f32_16x16x32_bf16 v[64:67], a[228:231], a[132:135], v[64:67]\n"
    "v_exp_f32 v6, v112\n"
    "v_mfma_f32_16x16x32_bf16 v[64:67], a[232:235], a[136:139], v[64:67]\n"
    "v_cvt_pk_bf16_f32 v145, v4, v5\n"
    "v_mfma_f32_16x16x32_bf16 v[64:67], a[236:239], a[140:143], v[64:67]\n"
    "s_add_u32 s72, s72, 0x8000\n"
    "buffer_load_dwordx4 a[192:195], v0, s[56:59], s72 offen offset:0 sc1\n"
    "v_mfma_f32_16x16x32_bf16 v[68:71], a[224:227], a[144:147], v[32:35]\n"
    "v_mfma_f32_16x16x32_bf16 v[68:71], a[228:231], a[148:151], v[68:71]\n"
    "v_exp_f32 v7, v113\n"
    "v_mfma_f32_16x16x32_bf16 v[68:71], a[232:235], a[152:155], v[68:71]\n"
    "v_exp_f32 v2, v114\n"
    "v_mfma_f32_16x16x32_bf16 v[68:71], a[236:239], a[156:159], v[68:71]\n"
    "buffer_load_dwordx4 a[196:199], v0, s[56:59], s72 offen offset:1024 sc1\n"
    "v_cvt_pk_bf16_f32 v146, v6, v7\n"
    "v_mfma_f32_16x16x32_bf16 v[72:75], a[224:227], a[160:163], v[32:35]\n"
    "v_mfma_f32_16x16x32_bf16 v[72:75], a[228:231], a[164:167], v[72:75]\n"
    "v_exp_f32 v3, v115\n"
    "v_mfma_f32_16x16x32_bf16 v[72:75], a[232:235], a[168:171], v[72:75]\n"
    "v_exp_f32 v4, v100\n"
    "v_mfma_f32_16x16x32_bf16 v[72:75], a[236:239], a[172:175], v[72:75]\n"
    "buffer_load_dwordx4 a[200:203], v0, s[56:59], s72 offen offset:2048 sc1\n"
    "v_cvt_pk_bf16_f32 v147, v2, v3\n"
    "v_mfma_f32_16x16x32_bf16 v[76:79], a[224:227], a[176:179], v[32:35]\n"
    "v_exp_f32 v5, v101\n"
    "v_mfma_f32_16x16x32_bf16 v[76:79], a[228:231], a[180:183], v[76:79]\n"
    "v_cvt_pk_bf16_f32 v148, v4, v5\n"
    "v_mfma_f32_16x16x32_bf16 v[76:79], a[232:235], a[184:187], v[76:79]\n"
    "v_exp_f32 v6, v102\n"
    "v_mfma_f32_16x16x32_bf16 v[76:79], a[236:239], a[188:191], v[76:79]\n"
    "buffer_load_dwordx4 a[204:207], v0, s[56:59], s72 offen offset:3072 sc1\n"
    "s_waitcnt vmcnt(12)\n"
    "v_mfma_f32_16x16x32_bf16 v[80:83], a[240:243], a[128:131], v[36:39]\n"
    "v_exp_f32 v7, v103\n"
    "v_mfma_f32_16x16x32_bf16 v[80:83], a[244:247], a[132:135], v[80:83]\n"
    "v_cvt_pk_bf16_f32 v149, v6, v7\n"
    "v_mfma_f32_16x16x32_bf16 v[80:83], a[248:251], a[136:139], v[80:83]\n"
    "v_exp_f32 v2, v116\n"
    "v_mfma_f32_16x16x32_bf16 v[80:83], a[252:255], a[140:143], v[80:83]\n"
    "buffer_load_dwordx4 a[208:211], v1, s[56:59], s72 offen offset:0 sc1\n"
    "v_mfma_f32_16x16x32_bf16 v[84:87], a[240:243], a[144:147], v[36:39]\n"
    "v_exp_f32 v3, v117\n"
    "v_mfma_f32_16x16x32_bf16 v[84:87], a[244:247], a[148:151], v[84:87]\n"
    "v_cvt_pk_bf16_f32 v150, v2, v3\n"
    "v_mfma_f32_16x16x32_bf16 v[84:87], a[248:251], a[152:155], v[84:87]\n"
    "v_exp_f32 v4, v118\n"
    "v_mfma_f32_16x16x32_bf16 v[84:87], a[252:255], a[156:159], v[84:87]\n"
    "buffer_load_dwordx4 a[212:215], v1, s[56:59], s72 offen offset:1024 sc1\n"
    "v_mfma_f32_16x16x32_bf16 v[88:91], a[240:243], a[160:163], v[36:39]\n"
    "v_exp_f32 v5, v119\n"
    "v_mfma_f32_16x16x32_bf16 v[88:91], a[244:247], a[164:167], v[88:91]\n"
    "v_cvt_pk_bf16_f32 v151, v4, v5\n"
    "v_mfma_f32_16x16x32_bf16 v[88:91], a[248:251], a[168:171], v[88:91]\n"
    "v_exp_f32 v6, v104\n"
    "v_mfma_f32_16x16x32_bf16 v[88:91], a[252:255], a[172:175], v[88:91]\n"
    "buffer_load_dwordx4 a[216:219], v1, s[56:59], s72 offen offset:2048 sc1\n"
    "v_mfma_f32_16x16x32_bf16 v[92:95], a[240:243], a[176:179], v[36:39]\n"
    "v_exp_f32 v7, v105\n"
    "v_mfma_f32_16x16x32_bf16 v[92:95], a[244:247], a[180:183], v[92:95]\n"
    "v_exp_f32 v2, v106\n"
    "v_mfma_f32_16x16x32_bf16 v[92:95], a[248:251], a[184:187], v[92:95]\n"
    "v_cvt_pk_bf16_f32 v152, v6, v7\n"
    "v_mfma_f32_16x16x32_bf16 v[92:95], a[252:255], a[188:191], v[92:95]\n"
    "buffer_load_dwordx4 a[220:223], v1, s[56:59], s72 offen offset:3072 sc1\n"
    "v_add_u32 v24, 0x8000, v24\n"
    "s_mov_b64 exec, 0xffff\n"
    "buffer_load_dwordx2 v[22:23], v24, s[56:59], 0 offen sc1\n"
    "s_mov_b64 exec, -1\n"
    "s_waitcnt vmcnt(13)\n"
    "v_mfma_f32_16x16x32_bf16 a[0:3], v[128:131], a[12:15], a[0:3]\n"
    "v_mfma_f32_16x16x32_bf16 a[4:7], v[132:135], a[12:15], a[4:7]\n"
    "v_mfma_f32_16x16x32_bf16 a[8:11], v[136:139], a[12:15], a[8:11]\n"
    "v_mfma_f32_16x16x32_bf16 v[160:163], v[140:143], a[12:15], v[160:163]\n"
    "buffer_load_dwordx4 v[192:195], v0, s[8:11], s72 offen offset:0 sc1\n"
    "v_mfma_f32_16x16x32_bf16 a[16:19], v[128:131], a[28:31], a[16:19]\n"
    "v_exp_f32 v3, v107\n"
    "v_mfma_f32_16x16x32_bf16 a[20:23], v[132:135], a[28:31], a[20:23]\n"
    "ds_read_b128 v[32:35], v9 offset:2112\n"
    "v_cvt_pk_bf16_f32 v153, v2, v3\n"
    "v_mfma_f32_16x16x32_bf16 a[24:27], v[136:139], a[28:31], a[24:27]\n"
    "v_exp_f32 v4, v120\n"
    "v_mfma_f32_16x16x32_bf16 v[164:167], v[140:143], a[28:31], v[164:167]\n"
    "buffer_load_dwordx4 v[196:199], v0, s[8:11], s72 offen offset:1024 sc1\n"
    "v_mfma_f32_16x16x32_bf16 a[32:35], v[128:131], a[44:47], a[32:35]\n"
    "v_exp_f32 v5, v121\n"
    "v_mfma_f32_16x16x32_bf16 a[36:39], v[132:135], a[44:47], a[36:39]\n"
    "ds_read_b128 v[36:39], v9 offset:2128\n"
    "v_cvt_pk_bf16_f32 v154, v4, v5\n"
    "v_mfma_f32_16x16x32_bf16 a[40:43], v[136:139], a[44:47], a[40:43]\n"
    "v_mfma_f32_16x16x32_bf16 v[168:171], v[140:143], a[44:47], v[168:171]\n"
    "buffer_load_dwordx4 v[200:203], v0, s[8:11], s72 offen offset:2048 sc1\n"
    "v_mfma_f32_16x16x32_bf16 a[48:51], v[128:131], a[60:63], a[48:51]\n"
    "v_exp_f32 v6, v122\n"
    "v_mfma_f32_16x16x32_bf16 a[52:55], v[132:135], a[60:63], a[52:55]\n"
    "ds_read_b128 v[60:63], v9 offset:1760\n"
    "v_mfma_f32_16x16x32_bf16 a[56:59], v[136:139], a[60:63], a[56:59]\n"
    "v_exp_f32 v7, v123\n"
    "v_mfma_f32_16x16x32_bf16 v[172:175], v[140:143], a[60:63], v[172:175]\n"
    "buffer_load_dwordx4 v[204:207], v0, s[8:11], s72 offen offset:3072 sc1\n"
    "v_cvt_pk_bf16_f32 v155, v6, v7\n"
    "s_waitcnt vmcnt(13)\n"
    "v_mfma_f32_16x16x32_bf16 a[64:67], v[128:131], a[76:79], a[64:67]\n"
    "v_exp_f32 v2, v108\n"
    "v_mfma_f32_16x16x32_bf16 a[68:71], v[132:135], a[76:79], a[68:71]\n"
    "v_exp_f32 v3, v109\n"
    "v_mfma_f32_16x16x32_bf16 a[72:75], v[136:139], a[76:79], a[72:75]\n"
    "v_exp_f32 v4, v110\n"
    "v_mfma_f32_16x16x32_bf16 v[176:179], v[140:143], a[76:79], v[176:179]\n"
    "buffer_load_dwordx4 v[208:211], v1, s[8:11], s72 offen offset:0 sc1\n"
    "v_mfma_f32_16x16x32_bf16 a[80:83], v[128:131], a[92:95], a[80:83]\n"
    "v_exp_f32 v5, v111\n"
    "v_mfma_f32_16x16x32_bf16 a[84:87], v[132:135], a[92:95], a[84:87]\n"
    "v_cvt_pk_bf16_f32 v156, v2, v3\n"
    "v_mfma_f32_16x16x32_bf16 a[88:91], v[136:139], a[92:95], a[88:91]\n"
    "v_exp_f32 v6, v124\n"
    "v_mfma_f32_16x16x32_bf16 v[180:183], v[140:143], a[92:95], v[180:183]\n"
    "buffer_load_dwordx4 v[212:215], v1, s[8:11], s72 offen offset:1024 sc1\n"
    "v_mfma_f32_16x16x32_bf16 a[96:99], v[128:131], a[108:111], a[96:99]\n"
    "v_exp_f32 v7, v125\n"
    "v_mfma_f32_16x16x32_bf16 a[100:103], v[132:135], a[108:111], a[100:103]\n"
    "v_cvt_pk_bf16_f32 v157, v4, v5\n"
    "v_mfma_f32_16x16x32_bf16 a[104:107], v[136:139], a[108:111], a[104:107]\n"
    "v_exp_f32 v2, v126\n"
    "v_mfma_f32_16x16x32_bf16 v[184:187], v[140:143], a[108:111], v[184:187]\n"
    "buffer_load_dwordx4 v[216:219], v1, s[8:11], s72 offen offset:2048 sc1\n"
    "v_mfma_f32_16x16x32_bf16 a[112:115], v[128:131], a[124:127], a[112:115]\n"
    "v_exp_f32 v3, v127\n"
    "v_mfma_f32_16x16x32_bf16 a[116:119], v[132:135], a[124:127], a[116:119]\n"
    "v_cvt_pk_bf16_f32 v158, v6, v7\n"
    "v_mfma_f32_16x16x32_bf16 a[120:123], v[136:139], a[124:127], a[120:123]\n"
    "v_cvt_pk_bf16_f32 v159, v2, v3\n"
    "v_mfma_f32_16x16x32_bf16 v[188:191], v[140:143], a[124:127], v[188:191]\n"
    "buffer_load_dwordx4 v[220:223], v1, s[8:11], s72 offen offset:3072 sc1\n"
    "s_waitcnt lgkmcnt(0)\n"
    "s_cbranch_vccnz .Lf_STUB10\n"
    ".Lf_BACK10:\n"
    "v_mfma_f32_16x16x32_bf16 v[10:13], v[60:63], v[144:147], v[10:13]\n"
    "v_exp_f32 v2, v64\n"
    "v_mfma_f32_16x16x32_bf16 v[14:17], v[60:63], v[148:151], v[14:17]\n"
    "v_exp_f32 v3, v65\n"
    "v_mfma_f32_16x16x32_bf16 v[40:43], v[60:63], v[152:155], v[40:43]\n"
    "v_exp_f32 v4, v66\n"
    "v_mfma_f32_16x16x32_bf16 v[44:47], v[60:63], v[156:159], v[44:47]\n"
    "v_exp_f32 v5, v67\n"
    "s_waitcnt vmcnt(13)\n"
    "v_mfma_f32_16x16x32_bf16 v[96:99], a[192:195], a[128:131], v[32:35]\n"
    "v_cvt_pk_bf16_f32 v128, v2, v3\n"
    "v_mfma_f32_16x16x32_bf16 v[96:99], a[196:199], a[132:135], v[96:99]\n"
    "v_exp_f32 v6, v80\n"
    "v_mfma_f32_16x16x32_bf16 v[96:99], a[200:203], a[136:139], v[96:99]\n"
    "v_cvt_pk_bf16_f32 v129, v4, v5\n"
    "v_mfma_f32_16x16x32_bf16 v[96:99], a[204:207], a[140:143], v[96:99]\n"
    "s_waitcnt vmcnt(8)\n"
    "v_cmp_ne_u64 s[88:89], s[74:75], v[22:23]\n"
    "s_and_b32 s88, s88, 0xffff\n"
    "s_cbranch_scc1 .Lf_PSTUB3\n"
    ".Lf_PBACK3:\n"
    "s_add_u32 s72, s72, 0x8000\n"
    "buffer_load_dwordx4 a[224:227], v0, s[56:59], s72 offen offset:0 sc1\n"
    "v_mfma_f32_16x16x32_bf16 v[100:103], a[192:195], a[144:147], v[32:35]\n"
    "v_mfma_f32_16x16x32_bf16 v[100:103], a[196:199], a[148:151], v[100:103]\n"
    "v_mfma_f32_16x16x32_bf16 v[100:103], a[200:203], a[152:155], v[100:103]\n"
    "v_mfma_f32_16x16x32_bf16 v[100:103], a[204:207], a[156:159], v[100:103]\n"
    "buffer_load_dwordx4 a[228:231], v0, s[56:59], s72 offen offset:1024 sc1\n"
    "v_mfma_f32_16x16x32_bf16 v[104:107], a[192:195], a[160:163], v[32:35]\n"
    "v_exp_f32 v7, v81\n"
    "v_mfma_f32_16x16x32_bf16 v[104:107], a[196:199], a[164:167], v[104:107]\n"
    "v_exp_f32 v2, v82\n"
    "v_mfma_f32_16x16x32_bf16 v[104:107], a[200:203], a[168:171], v[104:107]\n"
    "v_exp_f32 v3, v83\n"
    "v_mfma_f32_16x16x32_bf16 v[104:107], a[204:207], a[172:175], v[104:107]\n"
    "buffer_load_dwordx4 a[232:235], v0, s[56:59], s72 offen offset:2048 sc1\n"
    "v_cvt_pk_bf16_f32 v130, v6, v7\n"
    "v_mfma_f32_16x16x32_bf16 v[108:111], a[192:195], a[176:179], v[32:35]\n"
    "v_cvt_pk_bf16_f32 v131, v2, v3\n"
    "v_mfma_f32_16x16x32_bf16 v[108:111], a[196:199], a[180:183], v[108:111]\n"
    "v_exp_f32 v4, v68\n"
    "v_mfma_f32_16x16x32_bf16 v[108:111], a[200:203], a[184:187], v[108:111]\n"
    "v_mfma_f32_16x16x32_bf16 v[108:111], a[204:207], a[188:191], v[108:111]\n"
    "buffer_load_dwordx4 a[236:239], v0, s[56:59], s72 offen offset:3072 sc1\n"
    "v_mfma_f32_16x16x32_bf16 v[112:115], a[208:211], a[128:131], v[36:39]\n"
    "v_exp_f32 v5, v69\n"
    "v_mfma_f32_16x16x32_bf16 v[112:115], a[212:215], a[132:135], v[112:115]\n"
    "v_exp_f32 v6, v70\n"
    "v_mfma_f32_16x16x32_bf16 v[112:115], a[216:219], a[136:139], v[112:115]\n"
    "v_exp_f32 v7, v71\n"
    "v_mfma_f32_16x16x32_bf16 v[112:115], a[220:223], a[140:143], v[112:115]\n"
    "buffer_load_dwordx4 a[240:243], v1, s[56:59], s72 offen offset:0 sc1\n"
    "v_cvt_pk_bf16_f32 v132, v4, v5\n"
    "v_mfma_f32_16x16x32_bf16 v[116:119], a[208:211], a[144:147], v[36:39]\n"
    "v_cvt_pk_bf16_f32 v133, v6, v7\n"
    "v_mfma_f32_16x16x32_bf16 v[116:119], a[212:215], a[148:151], v[116:119]\n"
    "v_exp_f32 v2, v84\n"
    "v_mfma_f32_16x16x32_bf16 v[116:119], a[216:219], a[152:155], v[116:119]\n"
    "v_mfma_f32_16x16x32_bf16 v[116:119], a[220:223], a[156:159], v[116:119]\n"
    "buffer_load_dwordx4 a[244:247], v1, s[56:59], s72 offen offset:1024 sc1\n"
    "v_mfma_f32_16x16x32_bf16 v[120:123], a[208:211], a[160:163], v[36:39]\n"
    "v_exp_f32 v3, v85\n"
    "v_mfma_f32_16x16x32_bf16 v[120:123], a[212:215], a[164:167], v[120:123]\n"
    "v_exp_f32 v4, v86\n"
    "v_mfma_f32_16x16x32_bf16 v[120:123], a[216:219], a[168:171], v[120:123]\n"
    "v_exp_f32 v5, v87\n"
    "v_mfma_f32_16x16x32_bf16 v[120:123], a[220:223], a[172:175], v[120:123]\n"
    "buffer_load_dwordx4 a[248:251], v1, s[56:59], s72 offen offset:2048 sc1\n"
    "v_cvt_pk_bf16_f32 v134, v2, v3\n"
    "v_mfma_f32_16x16x32_bf16 v[124:127], a[208:211], a[176:179], v[36:39]\n"
    "v_cvt_pk_bf16_f32 v135, v4, v5\n"
    "v_mfma_f32_16x16x32_bf16 v[124:127], a[212:215], a[180:183], v[124:127]\n"
    "v_exp_f32 v6, v72\n"
    "v_mfma_f32_16x16x32_bf16 v[124:127], a[216:219], a[184:187], v[124:127]\n"
    "v_exp_f32 v7, v73\n"
    "v_mfma_f32_16x16x32_bf16 v[124:127], a[220:223], a[188:191], v[124:127]\n"
    "buffer_load_dwordx4 a[252:255], v1, s[56:59], s72 offen offset:3072 sc1\n"
    "s_waitcnt vmcnt(12)\n"
    "v_mfma_f32_16x16x32_bf16 a[0:3], v[144:147], v[192:195], a[0:3]\n"
    "v_exp_f32 v2, v74\n"
    "v_mfma_f32_16x16x32_bf16 a[4:7], v[148:151], v[192:195], a[4:7]\n"
    "v_cvt_pk_bf16_f32 v136, v6, v7\n"
    "v_mfma_f32_16x16x32_bf16 a[8:11], v[152:155], v[192:195], a[8:11]\n"
    "v_exp_f32 v3, v75\n"
    "v_mfma_f32_16x16x32_bf16 v[160:163], v[156:159], v[192:195], v[160:163]\n"
    "buffer_load_dwordx4 a[12:15], v0, s[8:11], s72 offen offset:0 sc1\n"
    "v_mfma_f32_16x16x32_bf16 a[16:19], v[144:147], v[196:199], a[16:19]\n"
    "v_exp_f32 v4, v88\n"
    "v_mfma_f32_16x16x32_bf16 a[20:23], v[148:151], v[196:199], a[20:23]\n"
    "ds_read_b128 v[32:35], v9 offset:2304\n"
    "v_mfma_f32_16x16x32_bf16 a[24:27], v[152:155], v[196:199], a[24:27]\n"
    "v_exp_f32 v5, v89\n"
    "v_mfma_f32_16x16x32_bf16 v[164:167], v[156:159], v[196:199], v[164:167]\n"
    "buffer_load_dwordx4 a[28:31], v0, s[8:11], s72 offen offset:1024 sc1\n"
    "v_mfma_f32_16x16x32_bf16 a[32:35], v[144:147], v[200:203], a[32:35]\n"
    "v_cvt_pk_bf16_f32 v137, v2, v3\n"
    "v_cvt_pk_bf16_f32 v138, v4, v5\n"
    "v_mfma_f32_16x16x32_bf16 a[36:39], v[148:151], v[200:203], a[36:39]\n"
    "ds_read_b128 v[36:39], v9 offset:2320\n"
    "v_mfma_f32_16x16x32_bf16 a[40:43], v[152:155], v[200:203], a[40:43]\n"
    "v_exp_f32 v6, v90\n"
    "v_mfma_f32_16x16x32_bf16 v[168:171], v[156:159], v[200:203], v[168:171]\n"
    "buffer_load_dwordx4 a[44:47], v0, s[8:11], s72 offen offset:2048 sc1\n"
    "v_mfma_f32_16x16x32_bf16 a[48:51], v[144:147], v[204:207], a[48:51]\n"
    "v_exp_f32 v7, v91\n"
    "v_mfma_f32_16x16x32_bf16 a[52:55], v[148:151], v[204:207], a[52:55]\n"
    "ds_read_b128 v[60:63], v9 offset:1952\n"
    "v_mfma_f32_16x16x32_bf16 a[56:59], v[152:155], v[204:207], a[56:59]\n"
    "v_exp_f32 v2, v76\n"
    "v_mfma_f32_16x16x32_bf16 v[172:175], v[156:159], v[204:207], v[172:175]\n"
    "buffer_load_dwordx4 a[60:63], v0, s[8:11], s72 offen offset:3072 sc1\n"
    "s_waitcnt vmcnt(12)\n"
    "v_mfma_f32_16x16x32_bf16 a[64:67], v[144:147], v[208:211], a[64:67]\n"
    "v_exp_f32 v3, v77\n"
    "v_mfma_f32_16x16x32_bf16 a[68:71], v[148:151], v[208:211], a[68:71]\n"
    "v_cvt_pk_bf16_f32 v139, v6, v7\n"
    "v_mfma_f32_16x16x32_bf16 a[72:75], v[152:155], v[208:211], a[72:75]\n"
    "v_exp_f32 v4, v78\n"
    "v_mfma_f32_16x16x32_bf16 v[176:179], v[156:159], v[208:211], v[176:179]\n"
    "buffer_load_dwordx4 a[76:79], v1, s[8:11], s72 offen offset:0 sc1\n"
    "v_mfma_f32_16x16x32_bf16 a[80:83], v[144:147], v[212:215], a[80:83]\n"
    "v_exp_f32 v5, v79\n"
    "v_mfma_f32_16x16x32_bf16 a[84:87], v[148:151], v[212:215], a[84:87]\n"
    "v_cvt_pk_bf16_f32 v140, v2, v3\n"
    "v_mfma_f32_16x16x32_bf16 a[88:91], v[152:155], v[212:215], a[88:91]\n"
    "v_exp_f32 v6, v92\n"
    "v_mfma_f32_16x16x32_bf16 v[180:183], v[156:159], v[212:215], v[180:183]\n"
    "buffer_load_dwordx4 a[92:95], v1, s[8:11], s72 offen offset:1024 sc1\n"
    "v_mfma_f32_16x16x32_bf16 a[96:99], v[144:147], v[216:219], a[96:99]\n"
    "v_exp_f32 v7, v93\n"
    "v_mfma_f32_16x16x32_bf16 a[100:103], v[148:151], v[216:219], a[100:103]\n"
    "v_cvt_pk_bf16_f32 v141, v4, v5\n"
    "v_mfma_f32_16x16x32_bf16 a[104:107], v[152:155], v[216:219], a[104:107]\n"
    "v_exp_f32 v2, v94\n"
    "v_mfma_f32_16x16x32_bf16 v[184:187], v[156:159], v[216:219], v[184:187]\n"
    "buffer_load_dwordx4 a[108:111], v1, s[8:11], s72 offen offset:2048 sc1\n"
    "v_mfma_f32_16x16x32_bf16 a[112:115], v[144:147], v[220:223], a[112:115]\n"
    "v_exp_f32 v3, v95\n"
    "v_mfma_f32_16x16x32_bf16 a[116:119], v[148:151], v[220:223], a[116:119]\n"
    "v_cvt_pk_bf16_f32 v142, v6, v7\n"
    "v_mfma_f32_16x16x32_bf16 a[120:123], v[152:155], v[220:223], a[120:123]\n"
    "v_cvt_pk_bf16_f32 v143, v2, v3\n"
    "v_mfma_f32_16x16x32_bf16 v[188:191], v[156:159], v[220:223], v[188:191]\n"
    "buffer_load_dwordx4 a[124:127], v1, s[8:11], s72 offen offset:3072 sc1\n"
    "s_waitcnt lgkmcnt(0)\n"
    "s_cbranch_vccnz .Lf_STUB11\n"
    ".Lf_BACK11:\n"
    "v_mfma_f32_16x16x32_bf16 v[10:13], v[60:63], v[128:131], v[10:13]\n"
    "v_exp_f32 v2, v96\n"
    "v_mfma_f32_16x16x32_bf16 v[14:17], v[60:63], v[132:135], v[14:17]\n"
    "v_exp_f32 v3, v97\n"
    "v_mfma_f32_16x16x32_bf16 v[40:43], v[60:63], v[136:139], v[40:43]\n"
    "v_exp_f32 v4, v98\n"
    "v_mfma_f32_16x16x32_bf16 v[44:47], v[60:63], v[140:143], v[44:47]\n"
    "v_cvt_pk_bf16_f32 v144, v2, v3\n"
    "s_waitcnt vmcnt(12)\n"
    "v_mfma_f32_16x16x32_bf16 v[64:67], a[224:227], a[128:131], v[32:35]\n"
    "v_exp_f32 v5, v99\n"
    "v_mfma_f32_16x16x32_bf16 v[64:67], a[228:231], a[132:135], v[64:67]\n"
    "v_cvt_pk_bf16_f32 v145, v4, v5\n"
    "v_mfma_f32_16x16x32_bf16 v[64:67], a[232:235], a[136:139], v[64:67]\n"
    "v_exp_f32 v6, v112\n"
    "v_mfma_f32_16x16x32_bf16 v[64:67], a[236:239], a[140:143], v[64:67]\n"
    "s_add_u32 s72, s72, 0x8000\n"
    "buffer_load_dwordx4 a[192:195], v0, s[56:59], s72 offen offset:0 sc1\n"
    "v_mfma_f32_16x16x32_bf16 v[68:71], a[224:227], a[144:147], v[32:35]\n"
    "v_mfma_f32_16x16x32_bf16 v[68:71], a[228:231], a[148:151], v[68:71]\n"
    "v_exp_f32 v7, v113\n"
    "v_mfma_f32_16x16x32_bf16 v[68:71], a[232:235], a[152:155], v[68:71]\n"
    "v_exp_f32 v2, v114\n"
    "v_mfma_f32_16x16x32_bf16 v[68:71], a[236:239], a[156:159], v[68:71]\n"
    "buffer_load_dwordx4 a[196:199], v0, s[56:59], s72 offen offset:1024 sc1\n"
    "v_mfma_f32_16x16x32_bf16 v[72:75], a[224:227], a[160:163], v[32:35]\n"
    "v_exp_f32 v3, v115\n"
    "v_mfma_f32_16x16x32_bf16 v[72:75], a[228:231], a[164:167], v[72:75]\n"
    "v_cvt_pk_bf16_f32 v146, v6, v7\n"
    "v_mfma_f32_16x16x32_bf16 v[72:75], a[232:235], a[168:171], v[72:75]\n"
    "v_exp_f32 v4, v100\n"
    "v_mfma_f32_16x16x32_bf16 v[72:75], a[236:239], a[172:175], v[72:75]\n"
    "buffer_load_dwordx4 a[200:203], v0, s[56:59], s72 offen offset:2048 sc1\n"
    "v_mfma_f32_16x16x32_bf16 v[76:79], a[224:227], a[176:179], v[32:35]\n"
    "v_exp_f32 v5, v101\n"
    "v_mfma_f32_16x16x32_bf16 v[76:79], a[228:231], a[180:183], v[76:79]\n"
    "v_cvt_pk_bf16_f32 v147, v2, v3\n"
    "v_mfma_f32_16x16x32_bf16 v[76:79], a[232:235], a[184:187], v[76:79]\n"
    "v_cvt_pk_bf16_f32 v148, v4, v5\n"
    "v_mfma_f32_16x16x32_bf16 v[76:79], a[236:239], a[188:191], v[76:79]\n"
    "buffer_load_dwordx4 a[204:207], v0, s[56:59], s72 offen offset:3072 sc1\n"
    "s_waitcnt vmcnt(12)\n"
    "v_mfma_f32_16x16x32_bf16 v[80:83], a[240:243], a[128:131], v[36:39]\n"
    "v_exp_f32 v6, v102\n"
    "v_mfma_f32_16x16x32_bf16 v[80:83], a[244:247], a[132:135], v[80:83]\n"
    "v_exp_f32 v7, v103\n"
    "v_mfma_f32_16x16x32_bf16 v[80:83], a[248:251], a[136:139], v[80:83]\n"
    "v_cvt_pk_bf16_f32 v149, v6, v7\n"
    "v_mfma_f32_16x16x32_bf16 v[80:83], a[252:255], a[140:143], v[80:83]\n"
    "buffer_load_dwordx4 a[208:211], v1, s[56:59], s72 offen offset:0 sc1\n"
    "v_mfma_f32_16x16x32_bf16 v[84:87], a[240:243], a[144:147], v[36:39]\n"
    "v_exp_f32 v2, v116\n"
    "v_mfma_f32_16x16x32_bf16 v[84:87], a[244:247], a[148:151], v[84:87]\n"
    "v_exp_f32 v3, v117\n"
    "v_mfma_f32_16x16x32_bf16 v[84:87], a[248:251], a[152:155], v[84:87]\n"
    "v_cvt_pk_bf16_f32 v150, v2, v3\n"
    "v_mfma_f32_16x16x32_bf16 v[84:87], a[252:255], a[156:159], v[84:87]\n"
    "buffer_load_dwordx4 a[212:215], v1, s[56:59], s72 offen offset:1024 sc1\n"
    "v_mfma_f32_16x16x32_bf16 v[88:91], a[240:243], a[160:163], v[36:39]\n"
    "v_exp_f32 v4, v118\n"
    "v_mfma_f32_16x16x32_bf16 v[88:91], a[244:247], a[164:167], v[88:91]\n"
    "v_exp_f32 v5, v119\n"
    "v_mfma_f32_16x16x32_bf16 v[88:91], a[248:251], a[168:171], v[88:91]\n"
    "v_cvt_pk_bf16_f32 v151, v4, v5\n"
    "v_mfma_f32_16x16x32_bf16 v[88:91], a[252:255], a[172:175], v[88:91]\n"
    "buffer_load_dwordx4 a[216:219], v1, s[56:59], s72 offen offset:2048 sc1\n"
    "v_mfma_f32_16x16x32_bf16 v[92:95], a[240:243], a[176:179], v[36:39]\n"
    "v_exp_f32 v6, v104\n"
    "v_mfma_f32_16x16x32_bf16 v[92:95], a[244:247], a[180:183], v[92:95]\n"
    "v_exp_f32 v7, v105\n"
    "v_mfma_f32_16x16x32_bf16 v[92:95], a[248:251], a[184:187], v[92:95]\n"
    "v_cvt_pk_bf16_f32 v152, v6, v7\n"
    "v_mfma_f32_16x16x32_bf16 v[92:95], a[252:255], a[188:191], v[92:95]\n"
    "buffer_load_dwordx4 a[220:223], v1, s[56:59], s72 offen offset:3072 sc1\n"
    "s_waitcnt vmcnt(12)\n"
    "v_mfma_f32_16x16x32_bf16 a[0:3], v[128:131], a[12:15], a[0:3]\n"
    "v_exp_f32 v2, v106\n"
    "v_mfma_f32_16x16x32_bf16 a[4:7], v[132:135], a[12:15], a[4:7]\n"
    "v_mfma_f32_16x16x32_bf16 a[8:11], v[136:139], a[12:15], a[8:11]\n"
    "v_exp_f32 v3, v107\n"
    "v_mfma_f32_16x16x32_bf16 v[160:163], v[140:143], a[12:15], v[160:163]\n"
    "buffer_load_dwordx4 v[192:195], v0, s[8:11], s72 offen offset:0 sc1\n"
    "v_cvt_pk_bf16_f32 v153, v2, v3\n"
    "v_mfma_f32_16x16x32_bf16 a[16:19], v[128:131], a[28:31], a[16:19]\n"
    "v_exp_f32 v4, v120\n"
    "v_mfma_f32_16x16x32_bf16 a[20:23], v[132:135], a[28:31], a[20:23]\n"
    "ds_read_b128 v[32:35], v9 offset:2496\n"
    "v_mfma_f32_16x16x32_bf16 a[24:27], v[136:139], a[28:31], a[24:27]\n"
    "v_exp_f32 v5, v121\n"
    "v_mfma_f32_16x16x32_bf16 v[164:167], v[140:143], a[28:31], v[164:167]\n"
    "buffer_load_dwordx4 v[196:199], v0, s[8:11], s72 offen offset:1024 sc1\n"
    "v_mfma_f32_16x16x32_bf16 a[32:35], v[128:131], a[44:47], a[32:35]\n"
    "v_exp_f32 v6, v122\n"
    "v_mfma_f32_16x16x32_bf16 a[36:39], v[132:135], a[44:47], a[36:39]\n"
    "ds_read_b128 v[36:39], v9 offset:2512\n"
    "v_mfma_f32_16x16x32_bf16 a[40:43], v[136:139], a[44:47], a[40:43]\n"
    "v_exp_f32 v7, v123\n"
    "v_mfma_f32_16x16x32_bf16 v[168:171], v[140:143], a[44:47], v[168:171]\n"
    "buffer_load_dwordx4 v[200:203], v0, s[8:11], s72 offen offset:2048 sc1\n"
    "v_mfma_f32_16x16x32_bf16 a[48:51], v[128:131], a[60:63], a[48:51]\n"
    "v_cvt_pk_bf16_f32 v154, v4, v5\n"
    "v_cvt_pk_bf16_f32 v155, v6, v7\n"
    "v_mfma_f32_16x16x32_bf16 a[52:55], v[132:135], a[60:63], a[52:55]\n"
    "ds_read_b128 v[60:63], v9 offset:2144\n"
    "v_mfma_f32_16x16x32_bf16 a[56:59], v[136:139], a[60:63], a[56:59]\n"
    "v_exp_f32 v2, v108\n"
    "v_mfma_f32_16x16x32_bf16 v[172:175], v[140:143], a[60:63], v[172:175]\n"
    "buffer_load_dwordx4 v[204:207], v0, s[8:11], s72 offen offset:3072 sc1\n"
    "s_waitcnt vmcnt(12)\n"
    "v_mfma_f32_16x16x32_bf16 a[64:67], v[128:131], a[76:79], a[64:67]\n"
    "v_mfma_f32_16x16x32_bf16 a[68:71], v[132:135], a[76:79], a[68:71]\n"
    "v_exp_f32 v3, v109\n"
    "v_mfma_f32_16x16x32_bf16 a[72:75], v[136:139], a[76:79], a[72:75]\n"
    "v_exp_f32 v4, v110\n"
    "v_mfma_f32_16x16x32_bf16 v[176:179], v[140:143], a[76:79], v[176:179]\n"
    "buffer_load_dwordx4 v[208:211], v1, s[8:11], s72 offen offset:0 sc1\n"
    "v_cvt_pk_bf16_f32 v156, v2, v3\n"
    "v_mfma_f32_16x16x32_bf16 a[80:83], v[128:131], a[92:95], a[80:83]\n"
    "v_mfma_f32_16x16x32_bf16 a[84:87], v[132:135], a[92:95], a[84:87]\n"
    "v_exp_f32 v5, v111\n"
    "v_mfma_f32_16x16x32_bf16 a[88:91], v[136:139], a[92:95], a[88:91]\n"
    "v_exp_f32 v6, v124\n"
    "v_mfma_f32_16x16x32_bf16 v[180:183], v[140:143], a[92:95], v[180:183]\n"
    "buffer_load_dwordx4 v[212:215], v1, s[8:11], s72 offen offset:1024 sc1\n"
    "v_cvt_pk_bf16_f32 v157, v4, v5\n"
    "v_mfma_f32_16x16x32_bf16 a[96:99], v[128:131], a[108:111], a[96:99]\n"
    "v_mfma_f32_16x16x32_bf16 a[100:103], v[132:135], a[108:111], a[100:103]\n"
    "v_exp_f32 v7, v125\n"
    "v_mfma_f32_16x16x32_bf16 a[104:107], v[136:139], a[108:111], a[104:107]\n"
    "v_exp_f32 v2, v126\n"
    "v_mfma_f32_16x16x32_bf16 v[184:187], v[140:143], a[108:111], v[184:187]\n"
    "buffer_load_dwordx4 v[216:219], v1, s[8:11], s72 offen offset:2048 sc1\n"
    "v_cvt_pk_bf16_f32 v158, v6, v7\n"
    "v_mfma_f32_16x16x32_bf16 a[112:115], v[128:131], a[124:127], a[112:115]\n"
    "v_mfma_f32_16x16x32_bf16 a[116:119], v[132:135], a[124:127], a[116:119]\n"
    "v_exp_f32 v3, v127\n"
    "v_mfma_f32_16x16x32_bf16 a[120:123], v[136:139], a[124:127], a[120:123]\n"
    "v_cvt_pk_bf16_f32 v159, v2, v3\n"
    "v_mfma_f32_16x16x32_bf16 v[188:191], v[140:143], a[124:127], v[188:191]\n"
    "buffer_load_dwordx4 v[220:223], v1, s[8:11], s72 offen offset:3072 sc1\n"
    "s_waitcnt lgkmcnt(0)\n"
    "s_cbranch_vccnz .Lf_STUB12\n"
    ".Lf_BACK12:\n"
    "v_mfma_f32_16x16x32_bf16 v[10:13], v[60:63], v[144:147], v[10:13]\n"
    "v_exp_f32 v2, v64\n"
    "v_mfma_f32_16x16x32_bf16 v[14:17], v[60:63], v[148:151], v[14:17]\n"
    "v_exp_f32 v3, v65\n"
    "v_mfma_f32_16x16x32_bf16 v[40:43], v[60:63], v[152:155], v[40:43]\n"
    "v_exp_f32 v4, v66\n"
    "v_mfma_f32_16x16x32_bf16 v[44:47], v[60:63], v[156:159], v[44:47]\n"
    "v_cvt_pk_bf16_f32 v128, v2, v3\n"
    "s_waitcnt vmcnt(12)\n"
    "v_mfma_f32_16x16x32_bf16 v[96:99], a[192:195], a[128:131], v[32:35]\n"
    "v_exp_f32 v5, v67\n"
    "v_mfma_f32_16x16x32_bf16 v[96:99], a[196:199], a[132:135], v[96:99]\n"
    "v_cvt_pk_bf16_f32 v129, v4, v5\n"
    "v_mfma_f32_16x16x32_bf16 v[96:99], a[200:203], a[136:139], v[96:99]\n"
    "v_exp_f32 v6, v80\n"
    "v_mfma_f32_16x16x32_bf16 v[96:99], a[204:207], a[140:143], v[96:99]\n"
    "s_add_u32 s72, s72, 0x8000\n"
    "buffer_load_dwordx4 a[224:227], v0, s[56:59], s72 offen offset:0 sc1\n"
    "v_mfma_f32_16x16x32_bf16 v[100:103], a[192:195], a[144:147], v[32:35]\n"
    "v_mfma_f32_16x16x32_bf16 v[100:103], a[196:199], a[148:151], v[100:103]\n"
    "v_exp_f32 v7, v81\n"
    "v_mfma_f32_16x16x32_bf16 v[100:103], a[200:203], a[152:155], v[100:103]\n"
    "v_exp_f32 v2, v82\n"
    "v_mfma_f32_16x16x32_bf16 v[100:103], a[204:207], a[156:159], v[100:103]\n"
    "buffer_load_dwordx4 a[228:231], v0, s[56:59], s72 offen offset:1024 sc1\n"
    "v_mfma_f32_16x16x32_bf16 v[104:107], a[192:195], a[160:163], v[32:35]\n"
    "v_exp_f32 v3, v83\n"
    "v_mfma_f32_16x16x32_bf16 v[104:107], a[196:199], a[164:167], v[104:107]\n"
    "v_cvt_pk_bf16_f32 v130, v6, v7\n"
    "v_mfma_f32_16x16x32_bf16 v[104:107], a[200:203], a[168:171], v[104:107]\n"
    "v_exp_f32 v4, v68\n"
    "v_mfma_f32_16x16x32_bf16 v[104:107], a[204:207], a[172:175], v[104:107]\n"
    "buffer_load_dwordx4 a[232:235], v0, s[56:59], s72 offen offset:2048 sc1\n"
    "v_mfma_f32_16x16x32_bf16 v[108:111], a[192:195], a[176:179], v[32:35]\n"
    "v_exp_f32 v5, v69\n"
    "v_mfma_f32_16x16x32_bf16 v[108:111], a[196:199], a[180:183], v[108:111]\n"
    "v_cvt_pk_bf16_f32 v131, v2, v3\n"
    "v_mfma_f32_16x16x32_bf16 v[108:111], a[200:203], a[184:187], v[108:111]\n"
    "v_cvt_pk_bf16_f32 v132, v4, v5\n"
    "v_mfma_f32_16x16x32_bf16 v[108:111], a[204:207], a[188:191], v[108:111]\n"
    "buffer_load_dwordx4 a[236:239], v0, s[56:59], s72 offen offset:3072 sc1\n"
    "s_waitcnt vmcnt(12)\n"
    "v_mfma_f32_16x16x32_bf16 v[112:115], a[208:211], a[128:131], v[36:39]\n"
    "v_exp_f32 v6, v70\n"
    "v_mfma_f32_16x16x32_bf16 v[112:115], a[212:215], a[132:135], v[112:115]\n"
    "v_exp_f32 v7, v71\n"
    "v_mfma_f32_16x16x32_bf16 v[112:115], a[216:219], a[136:139], v[112:115]\n"
    "v_cvt_pk_bf16_f32 v133, v6, v7\n"
    "v_mfma_f32_16x16x32_bf16 v[112:115], a[220:223], a[140:143], v[112:115]\n"
    "buffer_load_dwordx4 a[240:243], v1, s[56:59], s72 offen offset:0 sc1\n"
    "v_mfma_f32_16x16x32_bf16 v[116:119], a[208:211], a[144:147], v[36:39]\n"
    "v_exp_f32 v2, v84\n"
    "v_mfma_f32_16x16x32_bf16 v[116:119], a[212:215], a[148:151], v[116:119]\n"
    "v_exp_f32 v3, v85\n"
    "v_mfma_f32_16x16x32_bf16 v[116:119], a[216:219], a[152:155], v[116:119]\n"
    "v_cvt_pk_bf16_f32 v134, v2, v3\n"
    "v_mfma_f32_16x16x32_bf16 v[116:119], a[220:223], a[156:159], v[116:119]\n"
    "buffer_load_dwordx4 a[244:247], v1, s[56:59], s72 offen offset:1024 sc1\n"
    "v_mfma_f32_16x16x32_bf16 v[120:123], a[208:211], a[160:163], v[36:39]\n"
    "v_exp_f32 v4, v86\n"
    "v_mfma_f32_16x16x32_bf16 v[120:123], a[212:215], a[164:167], v[120:123]\n"
    "v_exp_f32 v5, v87\n"
    "v_mfma_f32_16x16x32_bf16 v[120:123], a[216:219], a[168:171], v[120:123]\n"
    "v_cvt_pk_bf16_f32 v135, v4, v5\n"
    "v_mfma_f32_16x16x32_bf16 v[120:123], a[220:223], a[172:175], v[120:123]\n"
    "buffer_load_dwordx4 a[248:251], v1, s[56:59], s72 offen offset:2048 sc1\n"
    "v_mfma_f32_16x16x32_bf16 v[124:127], a[208:211], a[176:179], v[36:39]\n"
    "v_exp_f32 v6, v72\n"
    "v_mfma_f32_16x16x32_bf16 v[124:127], a[212:215], a[180:183], v[124:127]\n"
    "v_exp_f32 v7, v73\n"
    "v_mfma_f32_16x16x32_bf16 v[124:127], a[216:219], a[184:187], v[124:127]\n"
    "v_cvt_pk_bf16_f32 v136, v6, v7\n"
    "v_mfma_f32_16x16x32_bf16 v[124:127], a[220:223], a[188:191], v[124:127]\n"
    "buffer_load_dwordx4 a[252:255], v1, s[56:59], s72 offen offset:3072 sc1\n"
    "s_waitcnt vmcnt(12)\n"
    "v_mfma_f32_16x16x32_bf16 a[0:3], v[144:147], v[192:195], a[0:3]\n"
    "v_exp_f32 v2, v74\n"
    "v_mfma_f32_16x16x32_bf16 a[4:7], v[148:151], v[192:195], a[4:7]\n"
    "v_mfma_f32_16x16x32_bf16 a[8:11], v[152:155], v[192:195], a[8:11]\n"
    "v_exp_f32 v3, v75\n"
    "v_mfma_f32_16x16x32_bf16 v[160:163], v[156:159], v[192:195], v[160:163]\n"
    "buffer_load_dwordx4 a[12:15], v0, s[8:11], s72 offen offset:0 sc1\n"
    "v_cvt_pk_bf16_f32 v137, v2, v3\n"
    "v_mfma_f32_16x16x32_bf16 a[16:19], v[144:147], v[196:199], a[16:19]\n"
    "v_exp_f32 v4, v88\n"
    "v_mfma_f32_16x16x32_bf16 a[20:23], v[148:151], v[196:199], a[20:23]\n"
    "ds_read_b128 v[32:35], v9 offset:2688\n"
    "v_mfma_f32_16x16x32_bf16 a[24:27], v[152:155], v[196:199], a[24:27]\n"
    "v_exp_f32 v5, v89\n"
    "v_mfma_f32_16x16x32_bf16 v[164:167], v[156:159], v[196:199], v[164:167]\n"
    "buffer_load_dwordx4 a[28:31], v0, s[8:11], s72 offen offset:1024 sc1\n"
    "v_mfma_f32_16x16x32_bf16 a[32:35], v[144:147], v[200:203], a[32:35]\n"
    "v_exp_f32 v6, v90\n"
    "v_mfma_f32_16x16x32_bf16 a[36:39], v[148:151], v[200:203], a[36:39]\n"
    "ds_read_b128 v[36:39], v9 offset:2704\n"
    "v_mfma_f32_16x16x32_bf16 a[40:43], v[152:155], v[200:203], a[40:43]\n"
    "v_exp_f32 v7, v91\n"
    "v_mfma_f32_16x16x32_bf16 v[168:171], v[156:159], v[200:203], v[168:171]\n"
    "buffer_load_dwordx4 a[44:47], v0, s[8:11], s72 offen offset:2048 sc1\n"
    "v_mfma_f32_16x16x32_bf16 a[48:51], v[144:147], v[204:207], a[48:51]\n"
    "v_cvt_pk_bf16_f32 v138, v4, v5\n"
    "v_cvt_pk_bf16_f32 v139, v6, v7\n"
    "v_mfma_f32_16x16x32_bf16 a[52:55], v[148:151], v[204:207], a[52:55]\n"
    "ds_read_b128 v[60:63], v9 offset:2336\n"
    "v_mfma_f32_16x16x32_bf16 a[56:59], v[152:155], v[204:207], a[56:59]\n"
    "v_exp_f32 v2, v76\n"
    "v_mfma_f32_16x16x32_bf16 v[172:175], v[156:159], v[204:207], v[172:175]\n"
    "buffer_load_dwordx4 a[60:63], v0, s[8:11], s72 offen offset:3072 sc1\n"
    "s_waitcnt vmcnt(12)\n"
    "v_mfma_f32_16x16x32_bf16 a[64:67], v[144:147], v[208:211], a[64:67]\n"
    "v_mfma_f32_16x16x32_bf16 a[68:71], v[148:151], v[208:211], a[68:71]\n"
    "v_exp_f32 v3, v77\n"
    "v_mfma_f32_16x16x32_bf16 a[72:75], v[152:155], v[208:211], a[72:75]\n"
    "v_exp_f32 v4, v78\n"
    "v_mfma_f32_16x16x32_bf16 v[176:179], v[156:159], v[208:211], v[176:179]\n"
    "buffer_load_dwordx4 a[76:79], v1, s[8:11], s72 offen offset:0 sc1\n"
    "v_cvt_pk_bf16_f32 v140, v2, v3\n"
    "v_mfma_f32_16x16x32_bf16 a[80:83], v[144:147], v[212:215], a[80:83]\n"
    "v_mfma_f32_16x16x32_bf16 a[84:87], v[148:151], v[212:215], a[84:87]\n"
    "v_exp_f32 v5, v79\n"
    "v_mfma_f32_16x16x32_bf16 a[88:91], v[152:155], v[212:215], a[88:91]\n"
    "v_exp_f32 v6, v92\n"
    "v_mfma_f32_16x16x32_bf16 v[180:183], v[156:159], v[212:215], v[180:183]\n"
    "buffer_load_dwordx4 a[92:95], v1, s[8:11], s72 offen offset:1024 sc1\n"
    "v_cvt_pk_bf16_f32 v141, v4, v5\n"
    "v_mfma_f32_16x16x32_bf16 a[96:99], v[144:147], v[216:219], a[96:99]\n"
    "v_mfma_f32_16x16x32_bf16 a[100:103], v[148:151], v[216:219], a[100:103]\n"
    "v_exp_f32 v7, v93\n"
    "v_mfma_f32_16x16x32_bf16 a[104:107], v[152:155], v[216:219], a[104:107]\n"
    "v_exp_f32 v2, v94\n"
    "v_mfma_f32_16x16x32_bf16 v[184:187], v[156:159], v[216:219], v[184:187]\n"
    "buffer_load_dwordx4 a[108:111], v1, s[8:11], s72 offen offset:2048 sc1\n"
    "v_cvt_pk_bf16_f32 v142, v6, v7\n"
    "v_mfma_f32_16x16x32_bf16 a[112:115], v[144:147], v[220:223], a[112:115]\n"
    "v_mfma_f32_16x16x32_bf16 a[116:119], v[148:151], v[220:223], a[116:119]\n"
    "v_exp_f32 v3, v95\n"
    "v_mfma_f32_16x16x32_bf16 a[120:123], v[152:155], v[220:223], a[120:123]\n"
    "v_cvt_pk_bf16_f32 v143, v2, v3\n"
    "v_mfma_f32_16x16x32_bf16 v[188:191], v[156:159], v[220:223], v[188:191]\n"
    "buffer_load_dwordx4 a[124:127], v1, s[8:11], s72 offen offset:3072 sc1\n"
    "s_waitcnt lgkmcnt(0)\n"
    "s_cbranch_vccnz .Lf_STUB13\n"
    ".Lf_BACK13:\n"
    "v_mfma_f32_16x16x32_bf16 v[10:13], v[60:63], v[128:131], v[10:13]\n"
    "v_exp_f32 v2, v96\n"
    "v_mfma_f32_16x16x32_bf16 v[14:17], v[60:63], v[132:135], v[14:17]\n"
    "v_exp_f32 v3, v97\n"
    "v_mfma_f32_16x16x32_bf16 v[40:43], v[60:63], v[136:139], v[40:43]\n"
    "v_exp_f32 v4, v98\n"
    "v_mfma_f32_16x16x32_bf16 v[44:47], v[60:63], v[140:143], v[44:47]\n"
    "v_exp_f32 v5, v99\n"
    "s_waitcnt vmcnt(12)\n"
    "v_mfma_f32_16x16x32_bf16 v[64:67], a[224:227], a[128:131], v[32:35]\n"
    "v_cvt_pk_bf16_f32 v144, v2, v3\n"
    "v_cvt_pk_bf16_f32 v145, v4, v5\n"
    "v_mfma_f32_16x16x32_bf16 v[64:67], a[228:231], a[132:135], v[64:67]\n"
    "v_and_b32 v29, 15, v25\n"
    "v_mfma_f32_16x16x32_bf16 v[64:67], a[232:235], a[136:139], v[64:67]\n"
    "v_exp_f32 v6, v112\n"
    "v_mfma_f32_16x16x32_bf16 v[64:67], a[236:239], a[140:143], v[64:67]\n"
    "s_add_u32 s72, s72, 0x8000\n"
    "buffer_load_dwordx4 a[192:195], v0, s[56:59], s72 offen offset:0 sc1\n"
    "v_mfma_f32_16x16x32_bf16 v[68:71], a[224:227], a[144:147], v[32:35]\n"
    "v_mfma_f32_16x16x32_bf16 v[68:71], a[228:231], a[148:151], v[68:71]\n"
    "v_mov_b32 v30, 126992\n"
    "v_mfma_f32_16x16x32_bf16 v[68:71], a[232:235], a[152:155], v[68:71]\n"
    "v_exp_f32 v7, v113\n"
    "v_mfma_f32_16x16x32_bf16 v[68:71], a[236:239], a[156:159], v[68:71]\n"
    "buffer_load_dwordx4 a[196:199], v0, s[56:59], s72 offen offset:1024 sc1\n"
    "v_cvt_pk_bf16_f32 v146, v6, v7\n"
    "v_mfma_f32_16x16x32_bf16 v[72:75], a[224:227], a[160:163], v[32:35]\n"
    "v_exp_f32 v2, v114\n"
    "v_mfma_f32_16x16x32_bf16 v[72:75], a[228:231], a[164:167], v[72:75]\n"
    "v_lshl_add_u32 v22, v29, 2, s68\n"
    "v_mfma_f32_16x16x32_bf16 v[72:75], a[232:235], a[168:171], v[72:75]\n"
    "v_exp_f32 v3, v115\n"
    "v_mfma_f32_16x16x32_bf16 v[72:75], a[236:239], a[172:175], v[72:75]\n"
    "buffer_load_dwordx4 a[200:203], v0, s[56:59], s72 offen offset:2048 sc1\n"
    "v_cvt_pk_bf16_f32 v147, v2, v3\n"
    "v_mfma_f32_16x16x32_bf16 v[76:79], a[224:227], a[176:179], v[32:35]\n"
    "v_exp_f32 v4, v100\n"
    "v_mfma_f32_16x16x32_bf16 v[76:79], a[228:231], a[180:183], v[76:79]\n"
    "v_lshl_add_u32 v23, v29, 2, s70\n"
    "v_mfma_f32_16x16x32_bf16 v[76:79], a[232:235], a[184:187], v[76:79]\n"
    "v_exp_f32 v5, v101\n"
    "v_mfma_f32_16x16x32_bf16 v[76:79], a[236:239], a[188:191], v[76:79]\n"
    "buffer_load_dwordx4 a[204:207], v0, s[56:59], s72 offen offset:3072 sc1\n"
    "v_cvt_pk_bf16_f32 v148, v4, v5\n"
    "s_waitcnt vmcnt(12)\n"
    "v_mfma_f32_16x16x32_bf16 v[80:83], a[240:243], a[128:131], v[36:39]\n"
    "v_exp_f32 v6, v102\n"
    "v_mfma_f32_16x16x32_bf16 v[80:83], a[244:247], a[132:135], v[80:83]\n"
    "v_lshl_add_u32 v24, v29, 2, s71\n"
    "v_mfma_f32_16x16x32_bf16 v[80:83], a[248:251], a[136:139], v[80:83]\n"
    "v_exp_f32 v7, v103\n"
    "v_mfma_f32_16x16x32_bf16 v[80:83], a[252:255], a[140:143], v[80:83]\n"
    "buffer_load_dwordx4 a[208:211], v1, s[56:59], s72 offen offset:0 sc1\n"
    "v_cvt_pk_bf16_f32 v149, v6, v7\n"
    "v_mfma_f32_16x16x32_bf16 v[84:87], a[240:243], a[144:147], v[36:39]\n"
    "v_mfma_f32_16x16x32_bf16 v[84:87], a[244:247], a[148:151], v[84:87]\n"
    "v_lshl_add_u32 v26, v29, 2, s86\n"
    "v_mfma_f32_16x16x32_bf16 v[84:87], a[248:251], a[152:155], v[84:87]\n"
    "v_exp_f32 v2, v116\n"
    "v_mfma_f32_16x16x32_bf16 v[84:87], a[252:255], a[156:159], v[84:87]\n"
    "buffer_load_dwordx4 a[212:215], v1, s[56:59], s72 offen offset:1024 sc1\n"
    "v_mfma_f32_16x16x32_bf16 v[88:91], a[240:243], a[160:163], v[36:39]\n"
    "v_exp_f32 v3, v117\n"
    "v_mfma_f32_16x16x32_bf16 v[88:91], a[244:247], a[164:167], v[88:91]\n"
    "v_add_u32 v27, s87, v0\n"
    "v_cvt_pk_bf16_f32 v150, v2, v3\n"
    "v_mfma_f32_16x16x32_bf16 v[88:91], a[248:251], a[168:171], v[88:91]\n"
    "v_exp_f32 v4, v118\n"
    "v_mfma_f32_16x16x32_bf16 v[88:91], a[252:255], a[172:175], v[88:91]\n"
    "buffer_load_dwordx4 a[216:219], v1, s[56:59], s72 offen offset:2048 sc1\n"
    "v_mfma_f32_16x16x32_bf16 v[92:95], a[240:243], a[176:179], v[36:39]\n"
    "v_exp_f32 v5, v119\n"
    "v_mfma_f32_16x16x32_bf16 v[92:95], a[244:247], a[180:183], v[92:95]\n"
    "v_and_b32 v28, 48, v25\n"
    "v_cvt_pk_bf16_f32 v151, v4, v5\n"
    "v_mfma_f32_16x16x32_bf16 v[92:95], a[248:251], a[184:187], v[92:95]\n"
    "v_exp_f32 v6, v104\n"
    "v_mfma_f32_16x16x32_bf16 v[92:95], a[252:255], a[188:191], v[92:95]\n"
    "buffer_load_dwordx4 a[220:223], v1, s[56:59], s72 offen offset:3072 sc1\n"
    "s_waitcnt vmcnt(12)\n"
    "v_mfma_f32_16x16x32_bf16 a[0:3], v[128:131], a[12:15], a[0:3]\n"
    "v_exp_f32 v7, v105\n"
    "v_mfma_f32_16x16x32_bf16 a[4:7], v[132:135], a[12:15], a[4:7]\n"
    "v_lshlrev_b32 v29, 4, v29\n"
    "v_cvt_pk_bf16_f32 v152, v6, v7\n"
    "v_mfma_f32_16x16x32_bf16 a[8:11], v[136:139], a[12:15], a[8:11]\n"
    "v_exp_f32 v2, v106\n"
    "v_mfma_f32_16x16x32_bf16 v[160:163], v[140:143], a[12:15], v[160:163]\n"
    "buffer_load_dwordx4 v[192:195], v0, s[8:11], s72 offen offset:0 sc1\n"
    "v_mfma_f32_16x16x32_bf16 a[16:19], v[128:131], a[28:31], a[16:19]\n"
    "v_exp_f32 v3, v107\n"
    "v_mfma_f32_16x16x32_bf16 a[20:23], v[132:135], a[28:31], a[20:23]\n"
    "v_lshl_add_u32 v29, v28, 6, v29\n"
    "ds_read_b128 v[32:35], v9 offset:2880\n"
    "v_mfma_f32_16x16x32_bf16 a[24:27], v[136:139], a[28:31], a[24:27]\n"
    "v_exp_f32 v4, v120\n"
    "v_mfma_f32_16x16x32_bf16 v[164:167], v[140:143], a[28:31], v[164:167]\n"
    "buffer_load_dwordx4 v[196:199], v0, s[8:11], s72 offen offset:1024 sc1\n"
    "v_mfma_f32_16x16x32_bf16 a[32:35], v[128:131], a[44:47], a[32:35]\n"
    "v_exp_f32 v5, v121\n"
    "v_mfma_f32_16x16x32_bf16 a[36:39], v[132:135], a[44:47], a[36:39]\n"
    "v_add_u32 v28, s95, v28\n"
    "ds_read_b128 v[36:39], v9 offset:2896\n"
    "v_mfma_f32_16x16x32_bf16 a[40:43], v[136:139], a[44:47], a[40:43]\n"
    "v_cvt_pk_bf16_f32 v153, v2, v3\n"
    "v_mfma_f32_16x16x32_bf16 v[168:171], v[140:143], a[44:47], v[168:171]\n"
    "buffer_load_dwordx4 v[200:203], v0, s[8:11], s72 offen offset:2048 sc1\n"
    "v_cvt_pk_bf16_f32 v154, v4, v5\n"
    "v_mfma_f32_16x16x32_bf16 a[48:51], v[128:131], a[60:63], a[48:51]\n"
    "v_mfma_f32_16x16x32_bf16 a[52:55], v[132:135], a[60:63], a[52:55]\n"
    "ds_read_b128 v[60:63], v9 offset:2528\n"
    "v_mfma_f32_16x16x32_bf16 a[56:59], v[136:139], a[60:63], a[56:59]\n"
    "v_exp_f32 v6, v122\n"
    "v_mfma_f32_16x16x32_bf16 v[172:175], v[140:143], a[60:63], v[172:175]\n"
    "buffer_load_dwordx4 v[204:207], v0, s[8:11], s72 offen offset:3072 sc1\n"
    "s_waitcnt vmcnt(12)\n"
    "v_mfma_f32_16x16x32_bf16 a[64:67], v[128:131], a[76:79], a[64:67]\n"
    "v_exp_f32 v7, v123\n"
    "v_mfma_f32_16x16x32_bf16 a[68:71], v[132:135], a[76:79], a[68:71]\n"
    "v_exp_f32 v2, v108\n"
    "v_mfma_f32_16x16x32_bf16 a[72:75], v[136:139], a[76:79], a[72:75]\n"
    "v_exp_f32 v3, v109\n"
    "v_mfma_f32_16x16x32_bf16 v[176:179], v[140:143], a[76:79], v[176:179]\n"
    "buffer_load_dwordx4 v[208:211], v1, s[8:11], s72 offen offset:0 sc1\n"
    "v_cvt_pk_bf16_f32 v155, v6, v7\n"
    "v_mfma_f32_16x16x32_bf16 a[80:83], v[128:131], a[92:95], a[80:83]\n"
    "v_exp_f32 v4, v110\n"
    "v_mfma_f32_16x16x32_bf16 a[84:87], v[132:135], a[92:95], a[84:87]\n"
    "v_exp_f32 v5, v111\n"
    "v_mfma_f32_16x16x32_bf16 a[88:91], v[136:139], a[92:95], a[88:91]\n"
    "v_cvt_pk_bf16_f32 v156, v2, v3\n"
    "v_cvt_pk_bf16_f32 v157, v4, v5\n"
    "v_mfma_f32_16x16x32_bf16 v[180:183], v[140:143], a[92:95], v[180:183]\n"
    "buffer_load_dwordx4 v[212:215], v1, s[8:11], s72 offen offset:1024 sc1\n"
    "v_mfma_f32_16x16x32_bf16 a[96:99], v[128:131], a[108:111], a[96:99]\n"
    "v_exp_f32 v6, v124\n"
    "v_mfma_f32_16x16x32_bf16 a[100:103], v[132:135], a[108:111], a[100:103]\n"
    "v_exp_f32 v7, v125\n"
    "v_mfma_f32_16x16x32_bf16 a[104:107], v[136:139], a[108:111], a[104:107]\n"
    "v_cvt_pk_bf16_f32 v158, v6, v7\n"
    "v_mfma_f32_16x16x32_bf16 v[184:187], v[140:143], a[108:111], v[184:187]\n"
    "buffer_load_dwordx4 v[216:219], v1, s[8:11], s72 offen offset:2048 sc1\n"
    "v_mfma_f32_16x16x32_bf16 a[112:115], v[128:131], a[124:127], a[112:115]\n"
    "v_exp_f32 v2, v126\n"
    "v_mfma_f32_16x16x32_bf16 a[116:119], v[132:135], a[124:127], a[116:119]\n"
    "v_exp_f32 v3, v127\n"
    "v_mfma_f32_16x16x32_bf16 a[120:123], v[136:139], a[124:127], a[120:123]\n"
    "v_cvt_pk_bf16_f32 v159, v2, v3\n"
    "v_mfma_f32_16x16x32_bf16 v[188:191], v[140:143], a[124:127], v[188:191]\n"
    "buffer_load_dwordx4 v[220:223], v1, s[8:11], s72 offen offset:3072 sc1\n"
    "s_waitcnt lgkmcnt(0)\n"
    "s_cbranch_vccnz .Lf_STUB14\n"
    ".Lf_BACK14:\n"
    "s_waitcnt vmcnt(15)\n"
    "v_mfma_f32_16x16x32_bf16 v[96:99], a[192:195], a[128:131], v[32:35]\n"
    "v_exp_f32 v2, v64\n"
    "s_waitcnt vmcnt(14)\n"
    "v_mfma_f32_16x16x32_bf16 v[96:99], a[196:199], a[132:135], v[96:99]\n"
    "v_exp_f32 v3, v65\n"
    "s_nop 0\n"
    "v_cvt_pk_bf16_f32 v128, v2, v3\n"
    "s_waitcnt vmcnt(13)\n"
    "v_mfma_f32_16x16x32_bf16 v[96:99], a[200:203], a[136:139], v[96:99]\n"
    "s_add_u32 s72, s72, 0x8000\n"
    "buffer_load_dwordx4 v[224:227], v0, s[8:11], s72 offen offset:0 sc1\n"
    "s_waitcnt vmcnt(13)\n"
    "v_mfma_f32_16x16x32_bf16 v[96:99], a[204:207], a[140:143], v[96:99]\n"
    "v_exp_f32 v4, v66\n"
    "v_mfma_f32_16x16x32_bf16 v[10:13], v[60:63], v[144:147], v[10:13]\n"
    "v_exp_f32 v5, v67\n"
    "v_exp_f32 v6, v80\n"
    "v_mfma_f32_16x16x32_bf16 v[100:103], a[192:195], a[144:147], v[32:35]\n"
    "s_add_u32 s65, s72, 0x8000\n"
    "buffer_load_dwordx4 a[224:227], v0, s[8:11], s65 offen offset:0 sc1\n"
    "v_mfma_f32_16x16x32_bf16 v[100:103], a[196:199], a[148:151], v[100:103]\n"
    "v_exp_f32 v7, v81\n"
    "v_cvt_pk_bf16_f32 v129, v4, v5\n"
    "v_mfma_f32_16x16x32_bf16 v[100:103], a[200:203], a[152:155], v[100:103]\n"
    "v_exp_f32 v2, v82\n"
    "v_mfma_f32_16x16x32_bf16 v[100:103], a[204:207], a[156:159], v[100:103]\n"
    "buffer_load_dwordx4 v[228:231], v0, s[8:11], s72 offen offset:1024 sc1\n"
    "v_exp_f32 v3, v83\n"
    "v_mfma_f32_16x16x32_bf16 v[14:17], v[60:63], v[148:151], v[14:17]\n"
    "v_cvt_pk_bf16_f32 v130, v6, v7\n"
    "v_exp_f32 v4, v68\n"
    "v_mfma_f32_16x16x32_bf16 v[104:107], a[192:195], a[160:163], v[32:35]\n"
    "v_exp_f32 v5, v69\n"
    "v_mfma_f32_16x16x32_bf16 v[104:107], a[196:199], a[164:167], v[104:107]\n"
    "buffer_load_dwordx4 a[228:231], v0, s[8:11], s65 offen offset:1024 sc1\n"
    "v_cvt_pk_bf16_f32 v131, v2, v3\n"
    "v_cvt_pk_bf16_f32 v132, v4, v5\n"
    "v_mfma_f32_16x16x32_bf16 v[104:107], a[200:203], a[168:171], v[104:107]\n"
    "v_exp_f32 v6, v70\n"
    "v_mfma_f32_16x16x32_bf16 v[104:107], a[204:207], a[172:175], v[104:107]\n"
    "v_exp_f32 v7, v71\n"
    "s_nop 0\n"
    "v_cvt_pk_bf16_f32 v133, v6, v7\n"
    "v_mfma_f32_16x16x32_bf16 v[40:43], v[60:63], v[152:155], v[40:43]\n"
    "buffer_load_dwordx4 v[232:235], v0, s[8:11], s72 offen offset:2048 sc1\n"
    "v_exp_f32 v2, v84\n"
    "s_waitcnt vmcnt(16)\n"
    "v_mfma_f32_16x16x32_bf16 v[112:115], a[208:211], a[128:131], v[36:39]\n"
    "v_exp_f32 v3, v85\n"
    "s_waitcnt vmcnt(15)\n"
    "v_mfma_f32_16x16x32_bf16 v[112:115], a[212:215], a[132:135], v[112:115]\n"
    "v_exp_f32 v4, v86\n"
    "v_cvt_pk_bf16_f32 v134, v2, v3\n"
    "s_waitcnt vmcnt(14)\n"
    "v_mfma_f32_16x16x32_bf16 v[112:115], a[216:219], a[136:139], v[112:115]\n"
    "buffer_load_dwordx4 a[232:235], v0, s[8:11], s65 offen offset:2048 sc1\n"
    "s_waitcnt vmcnt(14)\n"
    "v_mfma_f32_16x16x32_bf16 v[112:115], a[220:223], a[140:143], v[112:115]\n"
    "v_exp_f32 v5, v87\n"
    "v_exp_f32 v6, v72\n"
    "v_mfma_f32_16x16x32_bf16 v[44:47], v[60:63], v[156:159], v[44:47]\n"
    "v_exp_f32 v7, v73\n"
    "v_cvt_pk_bf16_f32 v135, v4, v5\n"
    "v_mfma_f32_16x16x32_bf16 v[116:119], a[208:211], a[144:147], v[36:39]\n"
    "buffer_load_dwordx4 v[236:239], v0, s[8:11], s72 offen offset:3072 sc1\n"
    "v_cvt_pk_bf16_f32 v136, v6, v7\n"
    "v_mfma_f32_16x16x32_bf16 v[116:119], a[212:215], a[148:151], v[116:119]\n"
    "v_exp_f32 v2, v74\n"
    "v_mfma_f32_16x16x32_bf16 v[116:119], a[216:219], a[152:155], v[116:119]\n"
    "v_exp_f32 v3, v75\n"
    "s_nop 0\n"
    "v_cvt_pk_bf16_f32 v137, v2, v3\n"
    "v_mfma_f32_16x16x32_bf16 v[116:119], a[220:223], a[156:159], v[116:119]\n"
    "buffer_load_dwordx4 a[236:239], v0, s[8:11], s65 offen offset:3072 sc1\n"
    "v_exp_f32 v4, v88\n"
    "v_mfma_f32_16x16x32_bf16 v[120:123], a[208:211], a[160:163], v[36:39]\n"
    "v_exp_f32 v5, v89\n"
    "s_nop 0\n"
    "v_cvt_pk_bf16_f32 v138, v4, v5\n"
    "v_mfma_f32_16x16x32_bf16 v[120:123], a[212:215], a[164:167], v[120:123]\n"
    "v_exp_f32 v6, v90\n"
    "v_mfma_f32_16x16x32_bf16 v[120:123], a[216:219], a[168:171], v[120:123]\n"
    "buffer_load_dwordx4 v[240:243], v1, s[8:11], s72 offen offset:0 sc1\n"
    "v_exp_f32 v7, v91\n"
    "v_mfma_f32_16x16x32_bf16 v[120:123], a[220:223], a[172:175], v[120:123]\n"
    "v_exp_f32 v2, v96\n"
    "v_mfma_f32_16x16x32_bf16 v[108:111], a[192:195], a[176:179], v[32:35]\n"
    "v_exp_f32 v3, v97\n"
    "v_cvt_pk_bf16_f32 v139, v6, v7\n"
    "v_mfma_f32_16x16x32_bf16 v[108:111], a[196:199], a[180:183], v[108:111]\n"
    "buffer_load_dwordx4 a[240:243], v1, s[8:11], s65 offen offset:0 sc1\n"
    "v_exp_f32 v4, v98\n"
    "v_mfma_f32_16x16x32_bf16 v[108:111], a[200:203], a[184:187], v[108:111]\n"
    "v_exp_f32 v5, v99\n"
    "v_mfma_f32_16x16x32_bf16 v[108:111], a[204:207], a[188:191], v[108:111]\n"
    "v_cvt_pk_bf16_f32 v48, v2, v3\n"
    "v_exp_f32 v6, v100\n"
    "v_mfma_f32_16x16x32_bf16 v[124:127], a[208:211], a[176:179], v[36:39]\n"
    "buffer_load_dwordx4 v[244:247], v1, s[8:11], s72 offen offset:1024 sc1\n"
    "v_cvt_pk_bf16_f32 v49, v4, v5\n"
    "v_mfma_f32_16x16x32_bf16 v[124:127], a[212:215], a[180:183], v[124:127]\n"
    "v_exp_f32 v7, v101\n"
    "s_nop 0\n"
    "v_cvt_pk_bf16_f32 v52, v6, v7\n"
    "v_mfma_f32_16x16x32_bf16 v[124:127], a[216:219], a[184:187], v[124:127]\n"
    "v_exp_f32 v2, v102\n"
    "v_mfma_f32_16x16x32_bf16 v[124:127], a[220:223], a[188:191], v[124:127]\n"
    "buffer_load_dwordx4 a[244:247], v1, s[8:11], s65 offen offset:1024 sc1\n"
    "v_exp_f32 v3, v103\n"
    "s_waitcnt vmcnt(19)\n"
    "v_mfma_f32_16x16x32_bf16 a[0:3], v[144:147], v[192:195], a[0:3]\n"
    "ds_read_b128 v[32:35], v9 offset:2720\n"
    "v_exp_f32 v4, v104\n"
    "v_mfma_f32_16x16x32_bf16 a[4:7], v[148:151], v[192:195], a[4:7]\n"
    "ds_read_b128 v[36:39], v9 offset:2912\n"
    "v_cvt_pk_bf16_f32 v53, v2, v3\n"
    "v_mfma_f32_16x16x32_bf16 a[8:11], v[152:155], v[192:195], a[8:11]\n"
    "buffer_load_dwordx4 v[248:251], v1, s[8:11], s72 offen offset:2048 sc1\n"
    "v_exp_f32 v5, v105\n"
    "s_waitcnt vmcnt(19)\n"
    "v_mfma_f32_16x16x32_bf16 a[16:19], v[144:147], v[196:199], a[16:19]\n"
    "v_exp_f32 v6, v106\n"
    "v_cvt_pk_bf16_f32 v56, v4, v5\n"
    "v_mfma_f32_16x16x32_bf16 a[20:23], v[148:151], v[196:199], a[20:23]\n"
    "v_exp_f32 v7, v107\n"
    "v_mfma_f32_16x16x32_bf16 a[24:27], v[152:155], v[196:199], a[24:27]\n"
    "buffer_load_dwordx4 a[248:251], v1, s[8:11], s65 offen offset:2048 sc1\n"
    "v_exp_f32 v2, v112\n"
    "s_waitcnt vmcnt(19)\n"
    "v_mfma_f32_16x16x32_bf16 a[32:35], v[144:147], v[200:203], a[32:35]\n"
    "v_exp_f32 v3, v113\n"
    "v_mfma_f32_16x16x32_bf16 a[36:39], v[148:151], v[200:203], a[36:39]\n"
    "v_cvt_pk_bf16_f32 v57, v6, v7\n"
    "v_exp_f32 v4, v114\n"
    "v_mfma_f32_16x16x32_bf16 a[40:43], v[152:155], v[200:203], a[40:43]\n"
    "buffer_load_dwordx4 v[252:255], v1, s[8:11], s72 offen offset:3072 sc1\n"
    "v_exp_f32 v5, v115\n"
    "s_waitcnt vmcnt(19)\n"
    "v_mfma_f32_16x16x32_bf16 a[48:51], v[144:147], v[204:207], a[48:51]\n"
    "v_cvt_pk_bf16_f32 v50, v2, v3\n"
    "v_cvt_pk_bf16_f32 v51, v4, v5\n"
    "v_mfma_f32_16x16x32_bf16 a[52:55], v[148:151], v[204:207], a[52:55]\n"
    "v_exp_f32 v6, v116\n"
    "v_mfma_f32_16x16x32_bf16 a[56:59], v[152:155], v[204:207], a[56:59]\n"
    "buffer_load_dwordx4 a[252:255], v1, s[8:11], s65 offen offset:3072 sc1\n"
    "v_exp_f32 v7, v117\n"
    "s_waitcnt vmcnt(19)\n"
    "v_mfma_f32_16x16x32_bf16 a[64:67], v[144:147], v[208:211], a[64:67]\n"
    "v_exp_f32 v2, v118\n"
    "v_cvt_pk_bf16_f32 v54, v6, v7\n"
    "v_mfma_f32_16x16x32_bf16 a[68:71], v[148:151], v[208:211], a[68:71]\n"
    "v_exp_f32 v3, v119\n"
    "s_nop 0\n"
    "v_cvt_pk_bf16_f32 v55, v2, v3\n"
    "v_mfma_f32_16x16x32_bf16 a[72:75], v[152:155], v[208:211], a[72:75]\n"
    "v_exp_f32 v4, v120\n"
    "s_waitcnt vmcnt(18)\n"
    "v_mfma_f32_16x16x32_bf16 a[80:83], v[144:147], v[212:215], a[80:83]\n"
    "v_exp_f32 v5, v121\n"
    "s_nop 0\n"
    "v_cvt_pk_bf16_f32 v58, v4, v5\n"
    "v_mfma_f32_16x16x32_bf16 a[84:87], v[148:151], v[212:215], a[84:87]\n"
    "v_exp_f32 v6, v122\n"
    "v_mfma_f32_16x16x32_bf16 a[88:91], v[152:155], v[212:215], a[88:91]\n"
    "v_exp_f32 v7, v123\n"
    "s_nop 0\n"
    "v_cvt_pk_bf16_f32 v59, v6, v7\n"
    "s_waitcnt vmcnt(17)\n"
    "v_mfma_f32_16x16x32_bf16 a[96:99], v[144:147], v[216:219], a[96:99]\n"
    "v_exp_f32 v2, v76\n"
    "v_mfma_f32_16x16x32_bf16 a[100:103], v[148:151], v[216:219], a[100:103]\n"
    "v_exp_f32 v3, v77\n"
    "v_mfma_f32_16x16x32_bf16 a[104:107], v[152:155], v[216:219], a[104:107]\n"
    "v_exp_f32 v4, v78\n"
    "s_waitcnt vmcnt(16)\n"
    "v_mfma_f32_16x16x32_bf16 a[112:115], v[144:147], v[220:223], a[112:115]\n"
    "v_exp_f32 v5, v79\n"
    "v_mfma_f32_16x16x32_bf16 a[116:119], v[148:151], v[220:223], a[116:119]\n"
    "v_cvt_pk_bf16_f32 v140, v2, v3\n"
    "v_cvt_pk_bf16_f32 v141, v4, v5\n"
    "v_mfma_f32_16x16x32_bf16 a[120:123], v[152:155], v[220:223], a[120:123]\n"
    "v_exp_f32 v6, v92\n"
    "s_cbranch_vccnz .Lf_TSTUB0\n"
    ".Lf_TBACK0:\n"
    "s_waitcnt vmcnt(15)\n"
    "v_mfma_f32_16x16x32_bf16 a[0:3], v[128:131], v[224:227], a[0:3]\n"
    "v_exp_f32 v7, v93\n"
    "v_mfma_f32_16x16x32_bf16 a[4:7], v[132:135], v[224:227], a[4:7]\n"
    "v_exp_f32 v2, v94\n"
    "v_mfma_f32_16x16x32_bf16 a[8:11], v[136:139], v[224:227], a[8:11]\n"
    "v_exp_f32 v3, v95\n"
    "s_waitcnt vmcnt(14)\n"
    "v_mfma_f32_16x16x32_bf16 a[0:3], v[48:51], a[224:227], a[0:3]\n"
    "v_cvt_pk_bf16_f32 v142, v6, v7\n"
    "v_cvt_pk_bf16_f32 v143, v2, v3\n"
    "v_mfma_f32_16x16x32_bf16 a[4:7], v[52:55], a[224:227], a[4:7]\n"
    "v_exp_f32 v4, v108\n"
    "v_mfma_f32_16x16x32_bf16 a[8:11], v[56:59], a[224:227], a[8:11]\n"
    "s_waitcnt lgkmcnt(0)\n"
    "v_exp_f32 v5, v109\n"
    "v_mfma_f32_16x16x32_bf16 v[10:13], v[32:35], v[128:131], v[10:13]\n"
    "v_exp_f32 v6, v110\n"
    "v_mfma_f32_16x16x32_bf16 v[14:17], v[32:35], v[132:135], v[14:17]\n"
    "v_exp_f32 v7, v111\n"
    "s_waitcnt vmcnt(13)\n"
    "v_mfma_f32_16x16x32_bf16 a[16:19], v[128:131], v[228:231], a[16:19]\n"
    "v_cvt_pk_bf16_f32 v60, v4, v5\n"
    "v_cvt_pk_bf16_f32 v61, v6, v7\n"
    "v_mfma_f32_16x16x32_bf16 a[20:23], v[132:135], v[228:231], a[20:23]\n"
    "ds_write_b128 v19, a[0:3] offset:0\n"
    "v_mfma_f32_16x16x32_bf16 a[24:27], v[136:139], v[228:231], a[24:27]\n"
    "v_exp_f32 v2, v124\n"
    "s_waitcnt vmcnt(12)\n"
    "v_mfma_f32_16x16x32_bf16 a[16:19], v[48:51], a[228:231], a[16:19]\n"
    "ds_write_b128 v20, a[4:7] offset:0\n"
    "v_mfma_f32_16x16x32_bf16 a[20:23], v[52:55], a[228:231], a[20:23]\n"
    "v_exp_f32 v3, v125\n"
    "v_mfma_f32_16x16x32_bf16 a[24:27], v[56:59], a[228:231], a[24:27]\n"
    "ds_write_b128 v21, a[8:11] offset:0\n"
    "v_mfma_f32_16x16x32_bf16 v[40:43], v[32:35], v[136:139], v[40:43]\n"
    "v_exp_f32 v4, v126\n"
    "v_mfma_f32_16x16x32_bf16 v[10:13], v[36:39], v[48:51], v[10:13]\n"
    "v_exp_f32 v5, v127\n"
    "s_waitcnt vmcnt(11)\n"
    "v_mfma_f32_16x16x32_bf16 a[32:35], v[128:131], v[232:235], a[32:35]\n"
    "v_cvt_pk_bf16_f32 v62, v2, v3\n"
    "v_cvt_pk_bf16_f32 v63, v4, v5\n"
    "v_mfma_f32_16x16x32_bf16 a[36:39], v[132:135], v[232:235], a[36:39]\n"
    "ds_write_b128 v19, a[16:19] offset:1024\n"
    "v_mfma_f32_16x16x32_bf16 a[40:43], v[136:139], v[232:235], a[40:43]\n"
    "s_waitcnt vmcnt(10)\n"
    "v_mfma_f32_16x16x32_bf16 a[32:35], v[48:51], a[232:235], a[32:35]\n"
    "ds_write_b128 v20, a[20:23] offset:1024\n"
    "v_mfma_f32_16x16x32_bf16 a[36:39], v[52:55], a[232:235], a[36:39]\n"
    "v_mfma_f32_16x16x32_bf16 a[40:43], v[56:59], a[232:235], a[40:43]\n"
    "ds_write_b32 v22, v10 offset:64\n"
    "ds_write_b128 v21, a[24:27] offset:1024\n"
    "v_mfma_f32_16x16x32_bf16 v[14:17], v[36:39], v[52:55], v[14:17]\n"
    "v_mfma_f32_16x16x32_bf16 v[40:43], v[36:39], v[56:59], v[40:43]\n"
    "s_waitcnt vmcnt(9)\n"
    "v_mfma_f32_16x16x32_bf16 a[48:51], v[128:131], v[236:239], a[48:51]\n"
    "v_mfma_f32_16x16x32_bf16 a[52:55], v[132:135], v[236:239], a[52:55]\n"
    "ds_write_b128 v19, a[32:35] offset:2048\n"
    "v_mfma_f32_16x16x32_bf16 a[56:59], v[136:139], v[236:239], a[56:59]\n"
    "s_waitcnt vmcnt(8)\n"
    "v_mfma_f32_16x16x32_bf16 a[48:51], v[48:51], a[236:239], a[48:51]\n"
    "ds_write_b128 v20, a[36:39] offset:2048\n"
    "v_mfma_f32_16x16x32_bf16 a[52:55], v[52:55], a[236:239], a[52:55]\n"
    "ds_write_b32 v23, v14 offset:64\n"
    "v_mfma_f32_16x16x32_bf16 a[56:59], v[56:59], a[236:239], a[56:59]\n"
    "ds_write_b32 v24, v40 offset:64\n"
    "ds_write_b128 v21, a[40:43] offset:2048\n"
    "s_waitcnt vmcnt(7)\n"
    "v_mfma_f32_16x16x32_bf16 a[64:67], v[128:131], v[240:243], a[64:67]\n"
    "v_mfma_f32_16x16x32_bf16 a[68:71], v[132:135], v[240:243], a[68:71]\n"
    "v_mfma_f32_16x16x32_bf16 a[72:75], v[136:139], v[240:243], a[72:75]\n"
    "s_waitcnt vmcnt(6)\n"
    "v_mfma_f32_16x16x32_bf16 a[64:67], v[48:51], a[240:243], a[64:67]\n"
    "ds_write_b128 v19, a[48:51] offset:3072\n"
    "v_mfma_f32_16x16x32_bf16 a[68:71], v[52:55], a[240:243], a[68:71]\n"
    "v_mfma_f32_16x16x32_bf16 a[72:75], v[56:59], a[240:243], a[72:75]\n"
    "ds_write_b128 v20, a[52:55] offset:3072\n"
    "s_cbranch_vccnz .Lf_TSTUB3\n"
    ".Lf_TBACK3:\n"
    "v_mfma_f32_16x16x32_bf16 v[44:47], v[32:35], v[140:143], v[44:47]\n"
    "v_mfma_f32_16x16x32_bf16 v[44:47], v[36:39], v[60:63], v[44:47]\n"
    "ds_write_b128 v21, a[56:59] offset:3072\n"
    "s_waitcnt vmcnt(5)\n"
    "v_mfma_f32_16x16x32_bf16 a[80:83], v[128:131], v[244:247], a[80:83]\n"
    "v_mfma_f32_16x16x32_bf16 a[84:87], v[132:135], v[244:247], a[84:87]\n"
    "ds_write_b128 v19, a[64:67] offset:4096\n"
    "v_mfma_f32_16x16x32_bf16 a[88:91], v[136:139], v[244:247], a[88:91]\n"
    "s_waitcnt lgkmcnt(1)\n"
    "s_barrier\n"
    "ds_read_b128 v[64:67], v27 offset:0\n"
    "ds_read_b128 v[68:71], v27 offset:8192\n"
    "ds_read_b128 v[72:75], v27 offset:16384\n"
    "s_waitcnt vmcnt(4)\n"
    "v_mfma_f32_16x16x32_bf16 a[80:83], v[48:51], a[244:247], a[80:83]\n"
    "ds_write_b128 v20, a[68:71] offset:4096\n"
    "ds_read_b128 v[80:83], v27 offset:1024\n"
    "ds_read_b128 v[84:87], v27 offset:9216\n"
    "ds_read_b128 v[88:91], v27 offset:17408\n"
    "v_mfma_f32_16x16x32_bf16 a[84:87], v[52:55], a[244:247], a[84:87]\n"
    "ds_read_b128 v[96:99], v27 offset:2048\n"
    "ds_read_b128 v[100:103], v27 offset:10240\n"
    "ds_read_b128 v[104:107], v27 offset:18432\n"
    "v_mfma_f32_16x16x32_bf16 a[88:91], v[56:59], a[244:247], a[88:91]\n"
    "ds_write_b32 v26, v44 offset:64\n"
    "ds_write_b128 v21, a[72:75] offset:4096\n"
    "ds_read_b128 v[112:115], v27 offset:3072\n"
    "ds_read_b128 v[116:119], v27 offset:11264\n"
    "ds_read_b128 v[120:123], v27 offset:19456\n"
    "s_waitcnt vmcnt(3)\n"
    "v_mfma_f32_16x16x32_bf16 a[96:99], v[128:131], v[248:251], a[96:99]\n"
    "ds_read_b128 v[76:79], v28 offset:64\n"
    "ds_read_b128 v[92:95], v28 offset:192\n"
    "v_mfma_f32_16x16x32_bf16 a[100:103], v[132:135], v[248:251], a[100:103]\n"
    "ds_read_b128 v[108:111], v28 offset:320\n"
    "ds_read_b128 v[124:127], v28 offset:448\n"
    "v_mfma_f32_16x16x32_bf16 a[104:107], v[136:139], v[248:251], a[104:107]\n"
    "s_waitcnt vmcnt(2)\n"
    "v_mfma_f32_16x16x32_bf16 a[96:99], v[48:51], a[248:251], a[96:99]\n"
    "ds_write_b128 v19, a[80:83] offset:5120\n"
    "v_mfma_f32_16x16x32_bf16 a[100:103], v[52:55], a[248:251], a[100:103]\n"
    "v_mfma_f32_16x16x32_bf16 a[104:107], v[56:59], a[248:251], a[104:107]\n"
    "ds_write_b128 v20, a[84:87] offset:5120\n"
    "s_waitcnt vmcnt(1)\n"
    "v_mfma_f32_16x16x32_bf16 a[112:115], v[128:131], v[252:255], a[112:115]\n"
    "v_mfma_f32_16x16x32_bf16 a[116:119], v[132:135], v[252:255], a[116:119]\n"
    "ds_write_b128 v21, a[88:91] offset:5120\n"
    "s_waitcnt lgkmcnt(3)\n"
    "v_add_f32 v2, v76, v92\n"
    "v_mfma_f32_16x16x32_bf16 a[120:123], v[136:139], v[252:255], a[120:123]\n"
    "v_add_f32 v3, v77, v93\n"
    "v_add_f32 v4, v78, v94\n"
    "v_add_f32 v5, v79, v95\n"
    "s_waitcnt vmcnt(0)\n"
    "v_mfma_f32_16x16x32_bf16 a[112:115], v[48:51], a[252:255], a[112:115]\n"
    "ds_write_b128 v19, a[96:99] offset:6144\n"
    "v_add_f32 v2, v2, v108\n"
    "v_mfma_f32_16x16x32_bf16 a[116:119], v[52:55], a[252:255], a[116:119]\n"
    "v_add_f32 v3, v3, v109\n"
    "v_add_f32 v4, v4, v110\n"
    "v_add_f32 v5, v5, v111\n"
    "v_mfma_f32_16x16x32_bf16 a[120:123], v[56:59], a[252:255], a[120:123]\n"
    "ds_write_b128 v20, a[100:103] offset:6144\n"
    "v_add_f32 v2, v2, v124\n"
    "v_mfma_f32_16x16x32_bf16 v[160:163], v[156:159], v[192:195], v[160:163]\n"
    "v_add_f32 v3, v3, v125\n"
    "v_add_f32 v4, v4, v126\n"
    "v_add_f32 v5, v5, v127\n"
    "v_mfma_f32_16x16x32_bf16 v[164:167], v[156:159], v[196:199], v[164:167]\n"
    "ds_write_b128 v21, a[104:107] offset:6144\n"
    "v_max3_u32 v6, v2, v3, v4\n"
    "v_mfma_f32_16x16x32_bf16 v[168:171], v[156:159], v[200:203], v[168:171]\n"
    "v_min3_u32 v7, v2, v3, v4\n"
    "v_max_u32 v6, v6, v5\n"
    "v_min_u32 v7, v7, v5\n"
    "v_mfma_f32_16x16x32_bf16 v[172:175], v[156:159], v[204:207], v[172:175]\n"
    "ds_write_b128 v19, a[112:115] offset:7168\n"
    "v_cmp_le_u32 s[88:89], 1.0, v6\n"
    "v_mfma_f32_16x16x32_bf16 v[176:179], v[156:159], v[208:211], v[176:179]\n"
    "v_cmp_gt_u32 vcc, 0x27800000, v7\n"
    "v_rcp_f32 v2, v2\n"
    "v_mfma_f32_16x16x32_bf16 v[180:183], v[156:159], v[212:215], v[180:183]\n"
    "ds_write_b128 v20, a[116:119] offset:7168\n"
    "v_mfma_f32_16x16x32_bf16 v[184:187], v[156:159], v[216:219], v[184:187]\n"
    "v_rcp_f32 v3, v3\n"
    "v_add_f32 v64, v64, v68\n"
    "v_mfma_f32_16x16x32_bf16 v[188:191], v[156:159], v[220:223], v[188:191]\n"
    "ds_write_b128 v21, a[120:123] offset:7168\n"
    "v_add_f32 v65, v65, v69\n"
    "v_mfma_f32_16x16x32_bf16 v[160:163], v[140:143], v[224:227], v[160:163]\n"
    "v_rcp_f32 v4, v4\n"
    "v_add_f32 v66, v66, v70\n"
    "v_mfma_f32_16x16x32_bf16 v[164:167], v[140:143], v[228:231], v[164:167]\n"
    "v_rcp_f32 v5, v5\n"
    "v_add_f32 v67, v67, v71\n"
    "v_mfma_f32_16x16x32_bf16 v[168:171], v[140:143], v[232:235], v[168:171]\n"
    "v_add_f32 v64, v64, v72\n"
    "v_add_f32 v65, v65, v73\n"
    "v_add_f32 v66, v66, v74\n"
    "s_or_b64 vcc, vcc, s[88:89]\n"
    "s_cbranch_scc1 .Lf_BAD\n"
    ".Lf_BADX:\n"
    "v_mfma_f32_16x16x32_bf16 v[172:175], v[140:143], v[236:239], v[172:175]\n"
    "s_waitcnt lgkmcnt(0)\n"
    "s_barrier\n"
    "ds_read_b32 v30, v30\n"
    "ds_read_b128 v[192:195], v27 offset:4096\n"
    "ds_read_b128 v[208:211], v27 offset:12288\n"
    "v_mfma_f32_16x16x32_bf16 v[176:179], v[140:143], v[240:243], v[176:179]\n"
    "ds_read_b128 v[76:79], v27 offset:20480\n"
    "ds_read_b128 v[196:199], v27 offset:5120\n"
    "ds_read_b128 v[212:215], v27 offset:13312\n"
    "v_mfma_f32_16x16x32_bf16 v[180:183], v[140:143], v[244:247], v[180:183]\n"
    "ds_read_b128 v[92:95], v27 offset:21504\n"
    "ds_read_b128 v[200:203], v27 offset:6144\n"
    "ds_read_b128 v[216:219], v27 offset:14336\n"
    "v_mfma_f32_16x16x32_bf16 v[184:187], v[140:143], v[248:251], v[184:187]\n"
    "ds_read_b128 v[108:111], v27 offset:22528\n"
    "ds_read_b128 v[204:207], v27 offset:7168\n"
    "ds_read_b128 v[220:223], v27 offset:15360\n"
    "v_mfma_f32_16x16x32_bf16 v[188:191], v[140:143], v[252:255], v[188:191]\n"
    "ds_read_b128 v[124:127], v27 offset:23552\n"
    "v_add_f32 v67, v67, v75\n"
    "v_add_f32 v80, v80, v84\n"
    "v_mfma_f32_16x16x32_bf16 v[160:163], v[60:63], a[224:227], v[160:163]\n"
    "v_add_f32 v81, v81, v85\n"
    "v_add_f32 v82, v82, v86\n"
    "v_add_f32 v83, v83, v87\n"
    "v_mfma_f32_16x16x32_bf16 v[164:167], v[60:63], a[228:231], v[164:167]\n"
    "v_add_f32 v80, v80, v88\n"
    "v_add_f32 v81, v81, v89\n"
    "v_add_f32 v82, v82, v90\n"
    "v_mfma_f32_16x16x32_bf16 v[168:171], v[60:63], a[232:235], v[168:171]\n"
    "v_add_f32 v83, v83, v91\n"
    "v_add_f32 v96, v96, v100\n"
    "v_add_f32 v97, v97, v101\n"
    "v_mfma_f32_16x16x32_bf16 v[172:175], v[60:63], a[236:239], v[172:175]\n"
    "v_add_f32 v98, v98, v102\n"
    "v_add_f32 v99, v99, v103\n"
    "v_add_f32 v96, v96, v104\n"
    "v_mfma_f32_16x16x32_bf16 v[176:179], v[60:63], a[240:243], v[176:179]\n"
    "v_add_f32 v97, v97, v105\n"
    "v_add_f32 v98, v98, v106\n"
    "v_add_f32 v99, v99, v107\n"
    "v_mfma_f32_16x16x32_bf16 v[180:183], v[60:63], a[244:247], v[180:183]\n"
    "v_add_f32 v112, v112, v116\n"
    "v_add_f32 v113, v113, v117\n"
    "v_add_f32 v114, v114, v118\n"
    "v_mfma_f32_16x16x32_bf16 v[184:187], v[60:63], a[248:251], v[184:187]\n"
    "v_add_f32 v115, v115, v119\n"
    "v_add_f32 v112, v112, v120\n"
    "v_add_f32 v113, v113, v121\n"
    "v_mfma_f32_16x16x32_bf16 v[188:191], v[60:63], a[252:255], v[188:191]\n"
    "v_add_f32 v114, v114, v122\n"
    "v_add_f32 v115, v115, v123\n"
    ".p2align 3\n"
    "s_waitcnt lgkmcnt(0)\n"
    "v_cmp_ne_u32_e64 vcc, 0, v30\n"
    "s_cbranch_vccnz .Lf_GEN\n"
    "v_pk_add_f32 v[192:193], v[192:193], v[208:209]\n"
    "v_pk_add_f32 v[196:197], v[196:197], v[212:213]\n"
    "v_pk_add_f32 v[200:201], v[200:201], v[216:217]\n"
    "v_pk_add_f32 v[204:205], v[204:205], v[220:221]\n"
    "v_pk_add_f32 v[64:65], v[64:65], v[160:161]\n"
    "v_pk_add_f32 v[80:81], v[80:81], v[164:165]\n"
    "v_pk_add_f32 v[96:97], v[96:97], v[168:169]\n"
    "v_pk_add_f32 v[112:113], v[112:113], v[172:173]\n"
    "v_pk_add_f32 v[192:193], v[192:193], v[176:177]\n"
    "v_pk_add_f32 v[196:197], v[196:197], v[180:181]\n"
    "v_pk_add_f32 v[200:201], v[200:201], v[184:185]\n"
    "v_pk_add_f32 v[204:205], v[204:205], v[188:189]\n"
    "v_pk_add_f32 v[192:193], v[192:193], v[76:77]\n"
    "v_pk_add_f32 v[196:197], v[196:197], v[92:93]\n"
    "v_pk_add_f32 v[200:201], v[200:201], v[108:109]\n"
    "v_pk_add_f32 v[204:205], v[204:205], v[124:125]\n"
    "v_pk_mul_f32 v[64:65], v[64:65], v[2:3]\n"
    "v_pk_mul_f32 v[80:81], v[80:81], v[2:3]\n"
    "v_pk_mul_f32 v[96:97], v[96:97], v[2:3]\n"
    "v_pk_mul_f32 v[112:113], v[112:113], v[2:3]\n"
    "v_pk_mul_f32 v[192:193], v[192:193], v[2:3]\n"
    "v_pk_mul_f32 v[196:197], v[196:197], v[2:3]\n"
    "v_pk_mul_f32 v[200:201], v[200:201], v[2:3]\n"
    "v_pk_mul_f32 v[204:205], v[204:205], v[2:3]\n"
    "v_cvt_pk_bf16_f32 v32, v64, v80\n"
    "v_cvt_pk_bf16_f32 v33, v96, v112\n"
    "v_cvt_pk_bf16_f32 v34, v192, v196\n"
    "v_cvt_pk_bf16_f32 v35, v200, v204\n"
    "buffer_store_dwordx4 v[32:35], v29, s[4:7], s94 offen offset:0 sc1\n"
    "v_cvt_pk_bf16_f32 v36, v65, v81\n"
    "v_cvt_pk_bf16_f32 v37, v97, v113\n"
    "v_cvt_pk_bf16_f32 v38, v193, v197\n"
    "v_cvt_pk_bf16_f32 v39, v201, v205\n"
    "buffer_store_dwordx4 v[36:39], v29, s[4:7], s94 offen offset:256 sc1\n"
    "v_pk_add_f32 v[194:195], v[194:195], v[210:211]\n"
    "v_pk_add_f32 v[198:199], v[198:199], v[214:215]\n"
    "v_pk_add_f32 v[202:203], v[202:203], v[218:219]\n"
    "v_pk_add_f32 v[206:207], v[206:207], v[222:223]\n"
    "v_pk_add_f32 v[66:67], v[66:67], v[162:163]\n"
    "v_pk_add_f32 v[82:83], v[82:83], v[166:167]\n"
    "v_pk_add_f32 v[98:99], v[98:99], v[170:171]\n"
    "v_pk_add_f32 v[114:115], v[114:115], v[174:175]\n"
    "v_pk_add_f32 v[194:195], v[194:195], v[178:179]\n"
    "v_pk_add_f32 v[198:199], v[198:199], v[182:183]\n"
    "v_pk_add_f32 v[202:203], v[202:203], v[186:187]\n"
    "v_pk_add_f32 v[206:207], v[206:207], v[190:191]\n"
    "v_pk_add_f32 v[194:195], v[194:195], v[78:79]\n"
    "v_pk_add_f32 v[198:199], v[198:199], v[94:95]\n"
    "v_pk_add_f32 v[202:203], v[202:203], v[110:111]\n"
    "v_pk_add_f32 v[206:207], v[206:207], v[126:127]\n"
    "v_pk_mul_f32 v[66:67], v[66:67], v[4:5]\n"
    "v_pk_mul_f32 v[82:83], v[82:83], v[4:5]\n"
    "v_pk_mul_f32 v[98:99], v[98:99], v[4:5]\n"
    "v_pk_mul_f32 v[114:115], v[114:115], v[4:5]\n"
    "v_pk_mul_f32 v[194:195], v[194:195], v[4:5]\n"
    "v_pk_mul_f32 v[198:199], v[198:199], v[4:5]\n"
    "v_pk_mul_f32 v[202:203], v[202:203], v[4:5]\n"
    "v_pk_mul_f32 v[206:207], v[206:207], v[4:5]\n"
    "v_cvt_pk_bf16_f32 v40, v66, v82\n"
    "v_cvt_pk_bf16_f32 v41, v98, v114\n"
    "v_cvt_pk_bf16_f32 v42, v194, v198\n"
    "v_cvt_pk_bf16_f32 v43, v202, v206\n"
    "buffer_store_dwordx4 v[40:43], v29, s[4:7], s94 offen offset:512 sc1\n"
    "v_cvt_pk_bf16_f32 v44, v67, v83\n"
    "v_cvt_pk_bf16_f32 v45, v99, v115\n"
    "v_cvt_pk_bf16_f32 v46, v195, v199\n"
    "v_cvt_pk_bf16_f32 v47, v203, v207\n"
    "buffer_store_dwordx4 v[44:47], v29, s[4:7], s94 offen offset:768 sc1\n"
    "s_endpgm\n"
    ".Lf_FSTUB2:\n"
    "v_mov_b32 v242, s74\n"
    "v_mov_b32 v243, s75\n"
    "v_mul_u32_u24 v241, 0x20000, v25\n"
    "s_add_u32 s88, s81, 0x10000\n"
    "s_mov_b64 exec, 0x1\n"
    "buffer_store_dwordx2 v[242:243], v241, s[56:59], s88 offen sc1\n"
    "s_mov_b64 exec, -1\n"
    "s_branch .Lf_FBACK2\n"
    ".Lf_FSTUB3:\n"
    "v_mov_b32 v242, s74\n"
    "v_mov_b32 v243, s75\n"
    "v_mul_u32_u24 v241, 0x20000, v25\n"
    "s_add_u32 s88, s81, 0x18000\n"
    "s_mov_b64 exec, 0x1\n"
    "buffer_store_dwordx2 v[242:243], v241, s[56:59], s88 offen sc1\n"
    "s_mov_b64 exec, -1\n"
    "s_branch .Lf_FBACK3\n"
    ".Lf_PSTUB1:\n"
    "s_call_b64 s[76:77], .Lf_SPIN\n"
    "s_branch .Lf_PBACK1\n"
    ".Lf_PSTUB2:\n"
    "s_call_b64 s[76:77], .Lf_SPIN\n"
    "s_branch .Lf_PBACK2\n"
    ".Lf_PSTUB3:\n"
    "s_call_b64 s[76:77], .Lf_SPIN\n"
    "s_branch .Lf_PBACK3\n"
    ".Lf_STUB1:\n"
    "s_mov_b32 s91, 0xff7fffff\n"
    "s_call_b64 s[76:77], .Lf_RARE0\n"
    "s_mov_b32 s91, 0xc1f00000\n"
    "s_branch .Lf_BACK1\n"
    ".Lf_STUB2:\n"
    "s_call_b64 s[76:77], .Lf_ADDM1\n"
    "s_branch .Lf_BACK2\n"
    ".Lf_STUB3:\n"
    "s_call_b64 s[76:77], .Lf_ADDM0\n"
    "s_branch .Lf_BACK3\n"
    ".Lf_DSK3:\n"
    "s_waitcnt vmcnt(0)\n"
    "s_branch .Lf_DBK3\n"
    ".Lf_STUB4:\n"
    "s_call_b64 s[76:77], .Lf_ADDM1\n"
    "s_branch .Lf_BACK4\n"
    ".Lf_STUB5:\n"
    "s_call_b64 s[76:77], .Lf_ADDM0\n"
    "s_branch .Lf_BACK5\n"
    ".Lf_STUB6:\n"
    "s_call_b64 s[76:77], .Lf_ADDM1\n"
    "s_branch .Lf_BACK6\n"
    ".Lf_STUB7:\n"
    "s_call_b64 s[76:77], .Lf_ADDM0\n"
    "s_branch .Lf_BACK7\n"
    ".Lf_STUB8:\n"
    "s_call_b64 s[76:77], .Lf_ADDM1\n"
    "s_branch .Lf_BACK8\n"
    ".Lf_STUB9:\n"
    "s_call_b64 s[76:77], .Lf_ADDM0\n"
    "s_branch .Lf_BACK9\n"
    ".Lf_STUB10:\n"
    "s_call_b64 s[76:77], .Lf_ADDM1\n"
    "s_branch .Lf_BACK10\n"
    ".Lf_STUB11:\n"
    "s_call_b64 s[76:77], .Lf_ADDM0\n"
    "s_branch .Lf_BACK11\n"
    ".Lf_STUB12:\n"
    "s_call_b64 s[76:77], .Lf_ADDM1\n"
    "s_branch .Lf_BACK12\n"
    ".Lf_STUB13:\n"
    "s_call_b64 s[76:77], .Lf_ADDM0\n"
    "s_branch .Lf_BACK13\n"
    ".Lf_STUB14:\n"
    "s_call_b64 s[76:77], .Lf_ADDM1\n"
    "s_branch .Lf_BACK14\n"
    ".Lf_TSTUB0:\n"
    "s_call_b64 s[76:77], .Lf_RARET0\n"
    "s_call_b64 s[76:77], .Lf_RARET1\n"
    "s_call_b64 s[76:77], .Lf_RARET2\n"
    "s_branch .Lf_TBACK0\n"
    ".Lf_TSTUB3:\n"
    "s_call_b64 s[76:77], .Lf_RARET3\n"
    "s_branch .Lf_TBACK3\n"
    ".Lf_BAD:\n"
    "ds_write_b32 v30, v30\n"
    "s_waitcnt lgkmcnt(0)\n"
    "s_branch .Lf_BADX\n"
    ".Lf_EUPD:\n"
    "s_waitcnt vmcnt(32)\n"
    ".Lf_EUPDC:\n"
    "v_cmp_ne_u64 vcc, s[74:75], v[224:225]\n"
    "v_cmp_ne_u64 s[88:89], s[74:75], v[226:227]\n"
    "s_or_b64 vcc, vcc, s[88:89]\n"
    "v_cmp_ne_u64 s[88:89], s[74:75], v[228:229]\n"
    "s_or_b64 vcc, vcc, s[88:89]\n"
    "v_cmp_ne_u64 s[88:89], s[74:75], v[230:231]\n"
    "s_or_b64 vcc, vcc, s[88:89]\n"
    "s_cbranch_scc0 .Lf_EUPDW\n"
    "s_sub_u32 s90, s90, 1\n"
    "s_cbranch_scc1 .Lf_EUPDW\n"
    "v_mul_u32_u24 v240, 128, v25\n"
    "buffer_load_dwordx2 v[224:225], v240, s[56:59], s69 offen sc1\n"
    "v_add_u32 v240, 8192, v240\n"
    "buffer_load_dwordx2 v[226:227], v240, s[56:59], s69 offen sc1\n"
    "v_add_u32 v240, 8192, v240\n"
    "buffer_load_dwordx2 v[228:229], v240, s[56:59], s69 offen sc1\n"
    "v_add_u32 v240, 8192, v240\n"
    "buffer_load_dwordx2 v[230:231], v240, s[56:59], s69 offen sc1\n"
    "v_add_u32 v240, 8192, v240\n"
    "s_waitcnt vmcnt(0)\n"
    "s_branch .Lf_EUPDC\n"
    ".Lf_EUPDW:\n"
    "s_cmp_lg_u32 s73, 0\n"
    "s_cselect_b64 vcc, -1, 0\n"
    "s_mov_b64 exec, 1\n"
    "v_mov_b32 v4, s74\n"
    "v_mov_b32 v5, s75\n"
    "buffer_store_dwordx2 v[4:5], off, s[56:59], 0 sc1\n"
    "s_mov_b64 exec, -1\n"
    "s_setpc_b64 s[76:77]\n"
    ".Lf_REDO:\n"
    "s_waitcnt vmcnt(0) lgkmcnt(0)\n"
    "s_barrier\n"
    "s_nop 15\n"
    "s_mov_b32 s73, 1\n"
    "s_mov_b64 vcc, -1\n"
    "s_sub_u32 s72, s72, 0x80000\n"
    "v_and_b32 v24, 15, v25\n"
    "v_lshlrev_b32 v24, 7, v24\n"
    "v_add_u32 v24, s44, v24\n"
    "v_lshrrev_b32 v9, 4, v25\n"
    "v_mad_u32_u24 v9, v9, 48, s64\n"
    "ds_read_b128 v[32:35], v9 offset:0\n"
    "ds_read_b128 v[36:39], v9 offset:16\n"
    "ds_read_b128 v[40:43], v9 offset:192\n"
    "ds_read_b128 v[44:47], v9 offset:208\n"
    "buffer_load_dwordx4 a[192:195], v0, s[56:59], s72 offen offset:0 sc1\n"
    "buffer_load_dwordx4 a[196:199], v0, s[56:59], s72 offen offset:1024 sc1\n"
    "buffer_load_dwordx4 a[200:203], v0, s[56:59], s72 offen offset:2048 sc1\n"
    "buffer_load_dwordx4 a[204:207], v0, s[56:59], s72 offen offset:3072 sc1\n"
    "buffer_load_dwordx4 a[208:211], v1, s[56:59], s72 offen offset:0 sc1\n"
    "buffer_load_dwordx4 a[212:215], v1, s[56:59], s72 offen offset:1024 sc1\n"
    "buffer_load_dwordx4 a[216:219], v1, s[56:59], s72 offen offset:2048 sc1\n"
    "buffer_load_dwordx4 a[220:223], v1, s[56:59], s72 offen offset:3072 sc1\n"
    "s_add_u32 s72, s72, 0x8000\n"
    "buffer_load_dwordx4 a[224:227], v0, s[56:59], s72 offen offset:0 sc1\n"
    "buffer_load_dwordx4 a[228:231], v0, s[56:59], s72 offen offset:1024 sc1\n"
    "buffer_load_dwordx4 a[232:235], v0, s[56:59], s72 offen offset:2048 sc1\n"
    "buffer_load_dwordx4 a[236:239], v0, s[56:59], s72 offen offset:3072 sc1\n"
    "buffer_load_dwordx4 a[240:243], v1, s[56:59], s72 offen offset:0 sc1\n"
    "buffer_load_dwordx4 a[244:247], v1, s[56:59], s72 offen offset:1024 sc1\n"
    "buffer_load_dwordx4 a[248:251], v1, s[56:59], s72 offen offset:2048 sc1\n"
    "buffer_load_dwordx4 a[252:255], v1, s[56:59], s72 offen offset:3072 sc1\n"
    "s_add_u32 s72, s72, 0x8000\n"
    "buffer_load_dwordx4 v[192:195], v0, s[56:59], s72 offen offset:0 sc1\n"
    "buffer_load_dwordx4 v[196:199], v0, s[56:59], s72 offen offset:1024 sc1\n"
    "buffer_load_dwordx4 v[200:203], v0, s[56:59], s72 offen offset:2048 sc1\n"
    "buffer_load_dwordx4 v[204:207], v0, s[56:59], s72 offen offset:3072 sc1\n"
    "buffer_load_dwordx4 v[208:211], v1, s[56:59], s72 offen offset:0 sc1\n"
    "buffer_load_dwordx4 v[212:215], v1, s[56:59], s72 offen offset:1024 sc1\n"
    "buffer_load_dwordx4 v[216:219], v1, s[56:59], s72 offen offset:2048 sc1\n"
    "buffer_load_dwordx4 v[220:223], v1, s[56:59], s72 offen offset:3072 sc1\n"
    "s_waitcnt vmcnt(0) lgkmcnt(0)\n"
    "s_branch .Lf_AGAIN\n"
    ".Lf_GEN:\n"
    "s_cmp_eq_u32 s73, 0\n"
    "s_cbranch_scc1 .Lf_REDO\n"
    "s_waitcnt lgkmcnt(0)\n"
    "v_mov_b32 v224, v160\n"
    "v_mov_b32 v225, v161\n"
    "v_mov_b32 v226, v162\n"
    "v_mov_b32 v227, v163\n"
    "v_mov_b32 v228, v164\n"
    "v_mov_b32 v229, v165\n"
    "v_mov_b32 v230, v166\n"
    "v_mov_b32 v231, v167\n"
    "v_mov_b32 v232, v168\n"
    "v_mov_b32 v233, v169\n"
    "v_mov_b32 v234, v170\n"
    "v_mov_b32 v235, v171\n"
    "v_mov_b32 v236, v172\n"
    "v_mov_b32 v237, v173\n"
    "v_mov_b32 v238, v174\n"
    "v_mov_b32 v239, v175\n"
    "v_mov_b32 v240, v176\n"
    "v_mov_b32 v241, v177\n"
    "v_mov_b32 v242, v178\n"
    "v_mov_b32 v243, v179\n"
    "v_mov_b32 v244, v180\n"
    "v_mov_b32 v245, v181\n"
    "v_mov_b32 v246, v182\n"
    "v_mov_b32 v247, v183\n"
    "v_mov_b32 v248, v184\n"
    "v_mov_b32 v249, v185\n"
    "v_mov_b32 v250, v186\n"
    "v_mov_b32 v251, v187\n"
    "v_mov_b32 v252, v188\n"
    "v_mov_b32 v253, v189\n"
    "v_mov_b32 v254, v190\n"
    "v_mov_b32 v255, v191\n"
    "ds_read_b128 v[160:163], v28 offset:0\n"
    "ds_read_b128 v[164:167], v28 offset:64\n"
    "ds_read_b128 v[168:171], v28 offset:128\n"
    "ds_read_b128 v[172:175], v28 offset:192\n"
    "ds_read_b128 v[176:179], v28 offset:256\n"
    "ds_read_b128 v[180:183], v28 offset:320\n"
    "ds_read_b128 v[184:187], v28 offset:384\n"
    "ds_read_b128 v[188:191], v28 offset:448\n"
    "ds_read_b128 v[64:67], v27 offset:0\n"
    "ds_read_b128 v[96:99], v27 offset:8192\n"
    "ds_read_b128 v[128:131], v27 offset:16384\n"
    "ds_read_b128 v[68:71], v27 offset:1024\n"
    "ds_read_b128 v[100:103], v27 offset:9216\n"
    "ds_read_b128 v[132:135], v27 offset:17408\n"
    "s_waitcnt lgkmcnt(6)\n"
    "v_min3_f32 v192, v160, v168, v176\n"
    "v_min3_f32 v193, v161, v169, v177\n"
    "v_min3_f32 v194, v162, v170, v178\n"
    "v_min3_f32 v195, v163, v171, v179\n"
    "ds_read_b128 v[72:75], v27 offset:2048\n"
    "v_min_f32 v192, v192, v184\n"
    "v_min_f32 v193, v193, v185\n"
    "v_min_f32 v194, v194, v186\n"
    "v_min_f32 v195, v195, v187\n"
    "ds_read_b128 v[104:107], v27 offset:10240\n"
    "v_sub_f32 v160, v192, v160\n"
    "v_sub_f32 v161, v193, v161\n"
    "v_sub_f32 v162, v194, v162\n"
    "v_sub_f32 v163, v195, v163\n"
    "ds_read_b128 v[136:139], v27 offset:18432\n"
    "v_sub_f32 v168, v192, v168\n"
    "v_sub_f32 v169, v193, v169\n"
    "v_sub_f32 v170, v194, v170\n"
    "v_sub_f32 v171, v195, v171\n"
    "ds_read_b128 v[76:79], v27 offset:3072\n"
    "v_sub_f32 v176, v192, v176\n"
    "v_sub_f32 v177, v193, v177\n"
    "v_sub_f32 v178, v194, v178\n"
    "v_sub_f32 v179, v195, v179\n"
    "ds_read_b128 v[108:111], v27 offset:11264\n"
    "v_sub_f32 v184, v192, v184\n"
    "v_sub_f32 v185, v193, v185\n"
    "v_sub_f32 v186, v194, v186\n"
    "v_sub_f32 v187, v195, v187\n"
    "ds_read_b128 v[140:143], v27 offset:19456\n"
    "v_exp_f32 v160, v160\n"
    "v_exp_f32 v161, v161\n"
    "v_exp_f32 v162, v162\n"
    "v_exp_f32 v163, v163\n"
    "ds_read_b128 v[80:83], v27 offset:4096\n"
    "v_exp_f32 v168, v168\n"
    "v_exp_f32 v169, v169\n"
    "v_exp_f32 v170, v170\n"
    "v_exp_f32 v171, v171\n"
    "ds_read_b128 v[112:115], v27 offset:12288\n"
    "v_exp_f32 v176, v176\n"
    "v_exp_f32 v177, v177\n"
    "v_exp_f32 v178, v178\n"
    "v_exp_f32 v179, v179\n"
    "ds_read_b128 v[144:147], v27 offset:20480\n"
    "v_exp_f32 v184, v184\n"
    "v_exp_f32 v185, v185\n"
    "v_exp_f32 v186, v186\n"
    "v_exp_f32 v187, v187\n"
    "v_pk_mul_f32 v[192:193], v[160:161], v[164:165]\n"
    "v_pk_mul_f32 v[194:195], v[162:163], v[166:167]\n"
    "v_pk_fma_f32 v[192:193], v[168:169], v[172:173], v[192:193]\n"
    "v_pk_fma_f32 v[194:195], v[170:171], v[174:175], v[194:195]\n"
    "v_pk_fma_f32 v[192:193], v[176:177], v[180:181], v[192:193]\n"
    "v_pk_fma_f32 v[194:195], v[178:179], v[182:183], v[194:195]\n"
    "v_pk_fma_f32 v[192:193], v[184:185], v[188:189], v[192:193]\n"
    "v_pk_fma_f32 v[194:195], v[186:187], v[190:191], v[194:195]\n"
    "v_rcp_f32 v192, v192\n"
    "v_rcp_f32 v193, v193\n"
    "v_rcp_f32 v194, v194\n"
    "v_rcp_f32 v195, v195\n"
    "s_nop 0\n"
    "v_pk_mul_f32 v[160:161], v[160:161], v[192:193]\n"
    "v_pk_mul_f32 v[162:163], v[162:163], v[194:195]\n"
    "v_pk_mul_f32 v[168:169], v[168:169], v[192:193]\n"
    "v_pk_mul_f32 v[170:171], v[170:171], v[194:195]\n"
    "v_pk_mul_f32 v[176:177], v[176:177], v[192:193]\n"
    "v_pk_mul_f32 v[178:179], v[178:179], v[194:195]\n"
    "v_pk_mul_f32 v[184:185], v[184:185], v[192:193]\n"
    "v_pk_mul_f32 v[186:187], v[186:187], v[194:195]\n"
    "v_pk_mul_f32 v[224:225], v[224:225], v[184:185]\n"
    "v_pk_mul_f32 v[226:227], v[226:227], v[186:187]\n"
    "v_pk_mul_f32 v[228:229], v[228:229], v[184:185]\n"
    "v_pk_mul_f32 v[230:231], v[230:231], v[186:187]\n"
    "v_pk_mul_f32 v[232:233], v[232:233], v[184:185]\n"
    "v_pk_mul_f32 v[234:235], v[234:235], v[186:187]\n"
    "v_pk_mul_f32 v[236:237], v[236:237], v[184:185]\n"
    "v_pk_mul_f32 v[238:239], v[238:239], v[186:187]\n"
    "v_pk_mul_f32 v[240:241], v[240:241], v[184:185]\n"
    "v_pk_mul_f32 v[242:243], v[242:243], v[186:187]\n"
    "v_pk_mul_f32 v[244:245], v[244:245], v[184:185]\n"
    "v_pk_mul_f32 v[246:247], v[246:247], v[186:187]\n"
    "v_pk_mul_f32 v[248:249], v[248:249], v[184:185]\n"
    "v_pk_mul_f32 v[250:251], v[250:251], v[186:187]\n"
    "v_pk_mul_f32 v[252:253], v[252:253], v[184:185]\n"
    "v_pk_mul_f32 v[254:255], v[254:255], v[186:187]\n"
    "s_waitcnt lgkmcnt(12)\n"
    "v_pk_fma_f32 v[224:225], v[64:65], v[160:161], v[224:225]\n"
    "v_pk_fma_f32 v[226:227], v[66:67], v[162:163], v[226:227]\n"
    "v_pk_fma_f32 v[224:225], v[96:97], v[168:169], v[224:225]\n"
    "v_pk_fma_f32 v[226:227], v[98:99], v[170:171], v[226:227]\n"
    "v_pk_fma_f32 v[224:225], v[128:129], v[176:177], v[224:225]\n"
    "v_pk_fma_f32 v[226:227], v[130:131], v[178:179], v[226:227]\n"
    "ds_read_b128 v[84:87], v27 offset:5120\n"
    "ds_read_b128 v[116:119], v27 offset:13312\n"
    "ds_read_b128 v[148:151], v27 offset:21504\n"
    "s_waitcnt lgkmcnt(12)\n"
    "v_pk_fma_f32 v[228:229], v[68:69], v[160:161], v[228:229]\n"
    "v_pk_fma_f32 v[230:231], v[70:71], v[162:163], v[230:231]\n"
    "v_pk_fma_f32 v[228:229], v[100:101], v[168:169], v[228:229]\n"
    "v_pk_fma_f32 v[230:231], v[102:103], v[170:171], v[230:231]\n"
    "v_pk_fma_f32 v[228:229], v[132:133], v[176:177], v[228:229]\n"
    "v_pk_fma_f32 v[230:231], v[134:135], v[178:179], v[230:231]\n"
    "ds_read_b128 v[88:91], v27 offset:6144\n"
    "ds_read_b128 v[120:123], v27 offset:14336\n"
    "ds_read_b128 v[152:155], v27 offset:22528\n"
    "s_waitcnt lgkmcnt(12)\n"
    "v_pk_fma_f32 v[232:233], v[72:73], v[160:161], v[232:233]\n"
    "v_pk_fma_f32 v[234:235], v[74:75], v[162:163], v[234:235]\n"
    "v_pk_fma_f32 v[232:233], v[104:105], v[168:169], v[232:233]\n"
    "v_pk_fma_f32 v[234:235], v[106:107], v[170:171], v[234:235]\n"
    "v_pk_fma_f32 v[232:233], v[136:137], v[176:177], v[232:233]\n"
    "v_pk_fma_f32 v[234:235], v[138:139], v[178:179], v[234:235]\n"
    "ds_read_b128 v[92:95], v27 offset:7168\n"
    "ds_read_b128 v[124:127], v27 offset:15360\n"
    "ds_read_b128 v[156:159], v27 offset:23552\n"
    "s_waitcnt lgkmcnt(12)\n"
    "v_pk_fma_f32 v[236:237], v[76:77], v[160:161], v[236:237]\n"
    "v_pk_fma_f32 v[238:239], v[78:79], v[162:163], v[238:239]\n"
    "v_pk_fma_f32 v[236:237], v[108:109], v[168:169], v[236:237]\n"
    "v_pk_fma_f32 v[238:239], v[110:111], v[170:171], v[238:239]\n"
    "v_pk_fma_f32 v[236:237], v[140:141], v[176:177], v[236:237]\n"
    "v_pk_fma_f32 v[238:239], v[142:143], v[178:179], v[238:239]\n"
    "s_waitcnt lgkmcnt(9)\n"
    "v_pk_fma_f32 v[240:241], v[80:81], v[160:161], v[240:241]\n"
    "v_pk_fma_f32 v[242:243], v[82:83], v[162:163], v[242:243]\n"
    "v_pk_fma_f32 v[240:241], v[112:113], v[168:169], v[240:241]\n"
    "v_pk_fma_f32 v[242:243], v[114:115], v[170:171], v[242:243]\n"
    "v_pk_fma_f32 v[240:241], v[144:145], v[176:177], v[240:241]\n"
    "v_pk_fma_f32 v[242:243], v[146:147], v[178:179], v[242:243]\n"
    "s_waitcnt lgkmcnt(6)\n"
    "v_pk_fma_f32 v[244:245], v[84:85], v[160:161], v[244:245]\n"
    "v_pk_fma_f32 v[246:247], v[86:87], v[162:163], v[246:247]\n"
    "v_pk_fma_f32 v[244:245], v[116:117], v[168:169], v[244:245]\n"
    "v_pk_fma_f32 v[246:247], v[118:119], v[170:171], v[246:247]\n"
    "v_pk_fma_f32 v[244:245], v[148:149], v[176:177], v[244:245]\n"
    "v_pk_fma_f32 v[246:247], v[150:151], v[178:179], v[246:247]\n"
    "s_waitcnt lgkmcnt(3)\n"
    "v_pk_fma_f32 v[248:249], v[88:89], v[160:161], v[248:249]\n"
    "v_pk_fma_f32 v[250:251], v[90:91], v[162:163], v[250:251]\n"
    "v_pk_fma_f32 v[248:249], v[120:121], v[168:169], v[248:249]\n"
    "v_pk_fma_f32 v[250:251], v[122:123], v[170:171], v[250:251]\n"
    "v_pk_fma_f32 v[248:249], v[152:153], v[176:177], v[248:249]\n"
    "v_pk_fma_f32 v[250:251], v[154:155], v[178:179], v[250:251]\n"
    "s_waitcnt lgkmcnt(0)\n"
    "v_pk_fma_f32 v[252:253], v[92:93], v[160:161], v[252:253]\n"
    "v_pk_fma_f32 v[254:255], v[94:95], v[162:163], v[254:255]\n"
    "v_pk_fma_f32 v[252:253], v[124:125], v[168:169], v[252:253]\n"
    "v_pk_fma_f32 v[254:255], v[126:127], v[170:171], v[254:255]\n"
    "v_pk_fma_f32 v[252:253], v[156:157], v[176:177], v[252:253]\n"
    "v_pk_fma_f32 v[254:255], v[158:159], v[178:179], v[254:255]\n"
    "v_cvt_pk_bf16_f32 v32, v224, v228\n"
    "v_cvt_pk_bf16_f32 v33, v232, v236\n"
    "v_cvt_pk_bf16_f32 v34, v240, v244\n"
    "v_cvt_pk_bf16_f32 v35, v248, v252\n"
    "buffer_store_dwordx4 v[32:35], v29, s[4:7], s94 offen offset:0 sc1\n"
    "v_cvt_pk_bf16_f32 v36, v225, v229\n"
    "v_cvt_pk_bf16_f32 v37, v233, v237\n"
    "v_cvt_pk_bf16_f32 v38, v241, v245\n"
    "v_cvt_pk_bf16_f32 v39, v249, v253\n"
    "buffer_store_dwordx4 v[36:39], v29, s[4:7], s94 offen offset:256 sc1\n"
    "v_cvt_pk_bf16_f32 v40, v226, v230\n"
    "v_cvt_pk_bf16_f32 v41, v234, v238\n"
    "v_cvt_pk_bf16_f32 v42, v242, v246\n"
    "v_cvt_pk_bf16_f32 v43, v250, v254\n"
    "buffer_store_dwordx4 v[40:43], v29, s[4:7], s94 offen offset:512 sc1\n"
    "v_cvt_pk_bf16_f32 v44, v227, v231\n"
    "v_cvt_pk_bf16_f32 v45, v235, v239\n"
    "v_cvt_pk_bf16_f32 v46, v243, v247\n"
    "v_cvt_pk_bf16_f32 v47, v251, v255\n"
    "buffer_store_dwordx4 v[44:47], v29, s[4:7], s94 offen offset:768 sc1\n"
    "s_endpgm\n"
    ".Lf_ADDM0:\n"
    "s_nop 15\n"
    "v_and_b32 v4, 15, v25\n"
    "v_lshl_add_u32 v4, v4, 2, s68\n"
    "ds_read_b32 v5, v4\n"
    "s_waitcnt lgkmcnt(0)\n"
    "v_add_f32 v96, v96, v5\n"
    "v_add_f32 v97, v97, v5\n"
    "v_add_f32 v98, v98, v5\n"
    "v_add_f32 v99, v99, v5\n"
    "v_add_f32 v112, v112, v5\n"
    "v_add_f32 v113, v113, v5\n"
    "v_add_f32 v114, v114, v5\n"
    "v_add_f32 v115, v115, v5\n"
    "v_and_b32 v4, 15, v25\n"
    "v_lshl_add_u32 v4, v4, 2, s70\n"
    "ds_read_b32 v5, v4\n"
    "s_waitcnt lgkmcnt(0)\n"
    "v_add_f32 v100, v100, v5\n"
    "v_add_f32 v101, v101, v5\n"
    "v_add_f32 v102, v102, v5\n"
    "v_add_f32 v103, v103, v5\n"
    "v_add_f32 v116, v116, v5\n"
    "v_add_f32 v117, v117, v5\n"
    "v_add_f32 v118, v118, v5\n"
    "v_add_f32 v119, v119, v5\n"
    "v_and_b32 v4, 15, v25\n"
    "v_lshl_add_u32 v4, v4, 2, s71\n"
    "ds_read_b32 v5, v4\n"
    "s_waitcnt lgkmcnt(0)\n"
    "v_add_f32 v104, v104, v5\n"
    "v_add_f32 v105, v105, v5\n"
    "v_add_f32 v106, v106, v5\n"
    "v_add_f32 v107, v107, v5\n"
    "v_add_f32 v120, v120, v5\n"
    "v_add_f32 v121, v121, v5\n"
    "v_add_f32 v122, v122, v5\n"
    "v_add_f32 v123, v123, v5\n"
    "v_and_b32 v4, 15, v25\n"
    "v_lshl_add_u32 v4, v4, 2, s86\n"
    "ds_read_b32 v5, v4\n"
    "s_waitcnt lgkmcnt(0)\n"
    "v_add_f32 v108, v108, v5\n"
    "v_add_f32 v109, v109, v5\n"
    "v_add_f32 v110, v110, v5\n"
    "v_add_f32 v111, v111, v5\n"
    "v_add_f32 v124, v124, v5\n"
    "v_add_f32 v125, v125, v5\n"
    "v_add_f32 v126, v126, v5\n"
    "v_add_f32 v127, v127, v5\n"
    "v_or3_b32 v18, v128, v129, 0\n"
    "v_or3_b32 v18, v130, v131, v18\n"
    "v_or3_b32 v18, v132, v133, v18\n"
    "v_or3_b32 v18, v134, v135, v18\n"
    "v_or3_b32 v18, v136, v137, v18\n"
    "v_or3_b32 v18, v138, v139, v18\n"
    "v_or3_b32 v18, v140, v141, v18\n"
    "v_or3_b32 v18, v142, v143, v18\n"
    "v_and_b32 v18, 0x40004000, v18\n"
    "v_cmp_ne_u32 vcc, 0, v18\n"
    "s_cbranch_vccnz .Lf_RARE0\n"
    "s_mov_b64 vcc, -1\n"
    "s_setpc_b64 s[76:77]\n"
    ".Lf_RARE0:\n"
    "s_nop 15\n"
    "s_mov_b32 s73, 1\n"
    "v_mov_b32 v2, 126992\n"
    "ds_write_b32 v2, v2\n"
    "s_waitcnt lgkmcnt(0)\n"
    "v_lshrrev_b32 v2, 4, v25\n"
    "v_lshlrev_b32 v2, 4, v2\n"
    "v_max3_f32 v18, v64, v65, s91\n"
    "v_max3_f32 v18, v18, v66, v67\n"
    "v_max3_f32 v18, v18, v80, v81\n"
    "v_max3_f32 v18, v18, v82, v83\n"
    "v_mov_b32 v3, v18\n"
    "s_nop 1\n"
    "v_permlane32_swap_b32 v18, v3\n"
    "s_nop 1\n"
    "v_max_f32 v18, v18, v3\n"
    "v_mov_b32 v3, v18\n"
    "s_nop 1\n"
    "v_permlane16_swap_b32 v18, v3\n"
    "s_nop 1\n"
    "v_max_f32 v18, v18, v3\n"
    "v_add_f32 v18, 0x41f00000, v18\n"
    "v_ceil_f32 v18, v18\n"
    "v_and_b32 v4, 15, v25\n"
    "v_lshl_add_u32 v4, v4, 2, s68\n"
    "ds_read_b32 v5, v4\n"
    "s_waitcnt lgkmcnt(0)\n"
    "v_sub_f32 v5, v5, v18\n"
    "ds_write_b32 v4, v5\n"
    "v_sub_f32 v64, v64, v18\n"
    "v_sub_f32 v65, v65, v18\n"
    "v_sub_f32 v66, v66, v18\n"
    "v_sub_f32 v67, v67, v18\n"
    "v_sub_f32 v80, v80, v18\n"
    "v_sub_f32 v81, v81, v18\n"
    "v_sub_f32 v82, v82, v18\n"
    "v_sub_f32 v83, v83, v18\n"
    "v_sub_f32 v96, v96, v18\n"
    "v_sub_f32 v97, v97, v18\n"
    "v_sub_f32 v98, v98, v18\n"
    "v_sub_f32 v99, v99, v18\n"
    "v_sub_f32 v112, v112, v18\n"
    "v_sub_f32 v113, v113, v18\n"
    "v_sub_f32 v114, v114, v18\n"
    "v_sub_f32 v115, v115, v18\n"
    "v_max_f32 v18, 0, v18\n"
    "v_exp_f32 v3, -v18\n"
    "s_waitcnt lgkmcnt(0)\n"
    "ds_bpermute_b32 v4, v2, v3 offset:0\n"
    "ds_bpermute_b32 v5, v2, v3 offset:4\n"
    "ds_bpermute_b32 v6, v2, v3 offset:8\n"
    "ds_bpermute_b32 v7, v2, v3 offset:12\n"
    "s_waitcnt lgkmcnt(0)\n"
    "v_mul_f32 v10, v10, v3\n"
    "v_accvgpr_read_b32 v8, a0\n"
    "v_mul_f32 v8, v8, v4\n"
    "v_accvgpr_write_b32 a0, v8\n"
    "v_accvgpr_read_b32 v9, a1\n"
    "v_mul_f32 v9, v9, v5\n"
    "v_accvgpr_write_b32 a1, v9\n"
    "v_accvgpr_read_b32 v8, a2\n"
    "v_mul_f32 v8, v8, v6\n"
    "v_accvgpr_write_b32 a2, v8\n"
    "v_accvgpr_read_b32 v9, a3\n"
    "v_mul_f32 v9, v9, v7\n"
    "v_accvgpr_write_b32 a3, v9\n"
    "v_accvgpr_read_b32 v8, a16\n"
    "v_mul_f32 v8, v8, v4\n"
    "v_accvgpr_write_b32 a16, v8\n"
    "v_accvgpr_read_b32 v9, a17\n"
    "v_mul_f32 v9, v9, v5\n"
    "v_accvgpr_write_b32 a17, v9\n"
    "v_accvgpr_read_b32 v8, a18\n"
    "v_mul_f32 v8, v8, v6\n"
    "v_accvgpr_write_b32 a18, v8\n"
    "v_accvgpr_read_b32 v9, a19\n"
    "v_mul_f32 v9, v9, v7\n"
    "v_accvgpr_write_b32 a19, v9\n"
    "v_accvgpr_read_b32 v8, a32\n"
    "v_mul_f32 v8, v8, v4\n"
    "v_accvgpr_write_b32 a32, v8\n"
    "v_accvgpr_read_b32 v9, a33\n"
    "v_mul_f32 v9, v9, v5\n"
    "v_accvgpr_write_b32 a33, v9\n"
    "v_accvgpr_read_b32 v8, a34\n"
    "v_mul_f32 v8, v8, v6\n"
    "v_accvgpr_write_b32 a34, v8\n"
    "v_accvgpr_read_b32 v9, a35\n"
    "v_mul_f32 v9, v9, v7\n"
    "v_accvgpr_write_b32 a35, v9\n"
    "v_accvgpr_read_b32 v8, a48\n"
    "v_mul_f32 v8, v8, v4\n"
    "v_accvgpr_write_b32 a48, v8\n"
    "v_accvgpr_read_b32 v9, a49\n"
    "v_mul_f32 v9, v9, v5\n"
    "v_accvgpr_write_b32 a49, v9\n"
    "v_accvgpr_read_b32 v8, a50\n"
    "v_mul_f32 v8, v8, v6\n"
    "v_accvgpr_write_b32 a50, v8\n"
    "v_accvgpr_read_b32 v9, a51\n"
    "v_mul_f32 v9, v9, v7\n"
    "v_accvgpr_write_b32 a51, v9\n"
    "v_accvgpr_read_b32 v8, a64\n"
    "v_mul_f32 v8, v8, v4\n"
    "v_accvgpr_write_b32 a64, v8\n"
    "v_accvgpr_read_b32 v9, a65\n"
    "v_mul_f32 v9, v9, v5\n"
    "v_accvgpr_write_b32 a65, v9\n"
    "v_accvgpr_read_b32 v8, a66\n"
    "v_mul_f32 v8, v8, v6\n"
    "v_accvgpr_write_b32 a66, v8\n"
    "v_accvgpr_read_b32 v9, a67\n"
    "v_mul_f32 v9, v9, v7\n"
    "v_accvgpr_write_b32 a67, v9\n"
    "v_accvgpr_read_b32 v8, a80\n"
    "v_mul_f32 v8, v8, v4\n"
    "v_accvgpr_write_b32 a80, v8\n"
    "v_accvgpr_read_b32 v9, a81\n"
    "v_mul_f32 v9, v9, v5\n"
    "v_accvgpr_write_b32 a81, v9\n"
    "v_accvgpr_read_b32 v8, a82\n"
    "v_mul_f32 v8, v8, v6\n"
    "v_accvgpr_write_b32 a82, v8\n"
    "v_accvgpr_read_b32 v9, a83\n"
    "v_mul_f32 v9, v9, v7\n"
    "v_accvgpr_write_b32 a83, v9\n"
    "v_accvgpr_read_b32 v8, a96\n"
    "v_mul_f32 v8, v8, v4\n"
    "v_accvgpr_write_b32 a96, v8\n"
    "v_accvgpr_read_b32 v9, a97\n"
    "v_mul_f32 v9, v9, v5\n"
    "v_accvgpr_write_b32 a97, v9\n"
    "v_accvgpr_read_b32 v8, a98\n"
    "v_mul_f32 v8, v8, v6\n"
    "v_accvgpr_write_b32 a98, v8\n"
    "v_accvgpr_read_b32 v9, a99\n"
    "v_mul_f32 v9, v9, v7\n"
    "v_accvgpr_write_b32 a99, v9\n"
    "v_accvgpr_read_b32 v8, a112\n"
    "v_mul_f32 v8, v8, v4\n"
    "v_accvgpr_write_b32 a112, v8\n"
    "v_accvgpr_read_b32 v9, a113\n"
    "v_mul_f32 v9, v9, v5\n"
    "v_accvgpr_write_b32 a113, v9\n"
    "v_accvgpr_read_b32 v8, a114\n"
    "v_mul_f32 v8, v8, v6\n"
    "v_accvgpr_write_b32 a114, v8\n"
    "v_accvgpr_read_b32 v9, a115\n"
    "v_mul_f32 v9, v9, v7\n"
    "v_accvgpr_write_b32 a115, v9\n"
    "v_max3_f32 v18, v68, v69, s91\n"
    "v_max3_f32 v18, v18, v70, v71\n"
    "v_max3_f32 v18, v18, v84, v85\n"
    "v_max3_f32 v18, v18, v86, v87\n"
    "v_mov_b32 v3, v18\n"
    "s_nop 1\n"
    "v_permlane32_swap_b32 v18, v3\n"
    "s_nop 1\n"
    "v_max_f32 v18, v18, v3\n"
    "v_mov_b32 v3, v18\n"
    "s_nop 1\n"
    "v_permlane16_swap_b32 v18, v3\n"
    "s_nop 1\n"
    "v_max_f32 v18, v18, v3\n"
    "v_add_f32 v18, 0x41f00000, v18\n"
    "v_ceil_f32 v18, v18\n"
    "v_and_b32 v4, 15, v25\n"
    "v_lshl_add_u32 v4, v4, 2, s70\n"
    "ds_read_b32 v5, v4\n"
    "s_waitcnt lgkmcnt(0)\n"
    "v_sub_f32 v5, v5, v18\n"
    "ds_write_b32 v4, v5\n"
    "v_sub_f32 v68, v68, v18\n"
    "v_sub_f32 v69, v69, v18\n"
    "v_sub_f32 v70, v70, v18\n"
    "v_sub_f32 v71, v71, v18\n"
    "v_sub_f32 v84, v84, v18\n"
    "v_sub_f32 v85, v85, v18\n"
    "v_sub_f32 v86, v86, v18\n"
    "v_sub_f32 v87, v87, v18\n"
    "v_sub_f32 v100, v100, v18\n"
    "v_sub_f32 v101, v101, v18\n"
    "v_sub_f32 v102, v102, v18\n"
    "v_sub_f32 v103, v103, v18\n"
    "v_sub_f32 v116, v116, v18\n"
    "v_sub_f32 v117, v117, v18\n"
    "v_sub_f32 v118, v118, v18\n"
    "v_sub_f32 v119, v119, v18\n"
    "v_max_f32 v18, 0, v18\n"
    "v_exp_f32 v3, -v18\n"
    "s_waitcnt lgkmcnt(0)\n"
    "ds_bpermute_b32 v4, v2, v3 offset:0\n"
    "ds_bpermute_b32 v5, v2, v3 offset:4\n"
    "ds_bpermute_b32 v6, v2, v3 offset:8\n"
    "ds_bpermute_b32 v7, v2, v3 offset:12\n"
    "s_waitcnt lgkmcnt(0)\n"
    "v_mul_f32 v14, v14, v3\n"
    "v_accvgpr_read_b32 v8, a4\n"
    "v_mul_f32 v8, v8, v4\n"
    "v_accvgpr_write_b32 a4, v8\n"
    "v_accvgpr_read_b32 v9, a5\n"
    "v_mul_f32 v9, v9, v5\n"
    "v_accvgpr_write_b32 a5, v9\n"
    "v_accvgpr_read_b32 v8, a6\n"
    "v_mul_f32 v8, v8, v6\n"
    "v_accvgpr_write_b32 a6, v8\n"
    "v_accvgpr_read_b32 v9, a7\n"
    "v_mul_f32 v9, v9, v7\n"
    "v_accvgpr_write_b32 a7, v9\n"
    "v_accvgpr_read_b32 v8, a20\n"
    "v_mul_f32 v8, v8, v4\n"
    "v_accvgpr_write_b32 a20, v8\n"
    "v_accvgpr_read_b32 v9, a21\n"
    "v_mul_f32 v9, v9, v5\n"
    "v_accvgpr_write_b32 a21, v9\n"
    "v_accvgpr_read_b32 v8, a22\n"
    "v_mul_f32 v8, v8, v6\n"
    "v_accvgpr_write_b32 a22, v8\n"
    "v_accvgpr_read_b32 v9, a23\n"
    "v_mul_f32 v9, v9, v7\n"
    "v_accvgpr_write_b32 a23, v9\n"
    "v_accvgpr_read_b32 v8, a36\n"
    "v_mul_f32 v8, v8, v4\n"
    "v_accvgpr_write_b32 a36, v8\n"
    "v_accvgpr_read_b32 v9, a37\n"
    "v_mul_f32 v9, v9, v5\n"
    "v_accvgpr_write_b32 a37, v9\n"
    "v_accvgpr_read_b32 v8, a38\n"
    "v_mul_f32 v8, v8, v6\n"
    "v_accvgpr_write_b32 a38, v8\n"
    "v_accvgpr_read_b32 v9, a39\n"
    "v_mul_f32 v9, v9, v7\n"
    "v_accvgpr_write_b32 a39, v9\n"
    "v_accvgpr_read_b32 v8, a52\n"
    "v_mul_f32 v8, v8, v4\n"
    "v_accvgpr_write_b32 a52, v8\n"
    "v_accvgpr_read_b32 v9, a53\n"
    "v_mul_f32 v9, v9, v5\n"
    "v_accvgpr_write_b32 a53, v9\n"
    "v_accvgpr_read_b32 v8, a54\n"
    "v_mul_f32 v8, v8, v6\n"
    "v_accvgpr_write_b32 a54, v8\n"
    "v_accvgpr_read_b32 v9, a55\n"
    "v_mul_f32 v9, v9, v7\n"
    "v_accvgpr_write_b32 a55, v9\n"
    "v_accvgpr_read_b32 v8, a68\n"
    "v_mul_f32 v8, v8, v4\n"
    "v_accvgpr_write_b32 a68, v8\n"
    "v_accvgpr_read_b32 v9, a69\n"
    "v_mul_f32 v9, v9, v5\n"
    "v_accvgpr_write_b32 a69, v9\n"
    "v_accvgpr_read_b32 v8, a70\n"
    "v_mul_f32 v8, v8, v6\n"
    "v_accvgpr_write_b32 a70, v8\n"
    "v_accvgpr_read_b32 v9, a71\n"
    "v_mul_f32 v9, v9, v7\n"
    "v_accvgpr_write_b32 a71, v9\n"
    "v_accvgpr_read_b32 v8, a84\n"
    "v_mul_f32 v8, v8, v4\n"
    "v_accvgpr_write_b32 a84, v8\n"
    "v_accvgpr_read_b32 v9, a85\n"
    "v_mul_f32 v9, v9, v5\n"
    "v_accvgpr_write_b32 a85, v9\n"
    "v_accvgpr_read_b32 v8, a86\n"
    "v_mul_f32 v8, v8, v6\n"
    "v_accvgpr_write_b32 a86, v8\n"
    "v_accvgpr_read_b32 v9, a87\n"
    "v_mul_f32 v9, v9, v7\n"
    "v_accvgpr_write_b32 a87, v9\n"
    "v_accvgpr_read_b32 v8, a100\n"
    "v_mul_f32 v8, v8, v4\n"
    "v_accvgpr_write_b32 a100, v8\n"
    "v_accvgpr_read_b32 v9, a101\n"
    "v_mul_f32 v9, v9, v5\n"
    "v_accvgpr_write_b32 a101, v9\n"
    "v_accvgpr_read_b32 v8, a102\n"
    "v_mul_f32 v8, v8, v6\n"
    "v_accvgpr_write_b32 a102, v8\n"
    "v_accvgpr_read_b32 v9, a103\n"
    "v_mul_f32 v9, v9, v7\n"
    "v_accvgpr_write_b32 a103, v9\n"
    "v_accvgpr_read_b32 v8, a116\n"
    "v_mul_f32 v8, v8, v4\n"
    "v_accvgpr_write_b32 a116, v8\n"
    "v_accvgpr_read_b32 v9, a117\n"
    "v_mul_f32 v9, v9, v5\n"
    "v_accvgpr_write_b32 a117, v9\n"
    "v_accvgpr_read_b32 v8, a118\n"
    "v_mul_f32 v8, v8, v6\n"
    "v_accvgpr_write_b32 a118, v8\n"
    "v_accvgpr_read_b32 v9, a119\n"
    "v_mul_f32 v9, v9, v7\n"
    "v_accvgpr_write_b32 a119, v9\n"
    "v_max3_f32 v18, v72, v73, s91\n"
    "v_max3_f32 v18, v18, v74, v75\n"
    "v_max3_f32 v18, v18, v88, v89\n"
    "v_max3_f32 v18, v18, v90, v91\n"
    "v_mov_b32 v3, v18\n"
    "s_nop 1\n"
    "v_permlane32_swap_b32 v18, v3\n"
    "s_nop 1\n"
    "v_max_f32 v18, v18, v3\n"
    "v_mov_b32 v3, v18\n"
    "s_nop 1\n"
    "v_permlane16_swap_b32 v18, v3\n"
    "s_nop 1\n"
    "v_max_f32 v18, v18, v3\n"
    "v_add_f32 v18, 0x41f00000, v18\n"
    "v_ceil_f32 v18, v18\n"
    "v_and_b32 v4, 15, v25\n"
    "v_lshl_add_u32 v4, v4, 2, s71\n"
    "ds_read_b32 v5, v4\n"
    "s_waitcnt lgkmcnt(0)\n"
    "v_sub_f32 v5, v5, v18\n"
    "ds_write_b32 v4, v5\n"
    "v_sub_f32 v72, v72, v18\n"
    "v_sub_f32 v73, v73, v18\n"
    "v_sub_f32 v74, v74, v18\n"
    "v_sub_f32 v75, v75, v18\n"
    "v_sub_f32 v88, v88, v18\n"
    "v_sub_f32 v89, v89, v18\n"
    "v_sub_f32 v90, v90, v18\n"
    "v_sub_f32 v91, v91, v18\n"
    "v_sub_f32 v104, v104, v18\n"
    "v_sub_f32 v105, v105, v18\n"
    "v_sub_f32 v106, v106, v18\n"
    "v_sub_f32 v107, v107, v18\n"
    "v_sub_f32 v120, v120, v18\n"
    "v_sub_f32 v121, v121, v18\n"
    "v_sub_f32 v122, v122, v18\n"
    "v_sub_f32 v123, v123, v18\n"
    "v_max_f32 v18, 0, v18\n"
    "v_exp_f32 v3, -v18\n"
    "s_waitcnt lgkmcnt(0)\n"
    "ds_bpermute_b32 v4, v2, v3 offset:0\n"
    "ds_bpermute_b32 v5, v2, v3 offset:4\n"
    "ds_bpermute_b32 v6, v2, v3 offset:8\n"
    "ds_bpermute_b32 v7, v2, v3 offset:12\n"
    "s_waitcnt lgkmcnt(0)\n"
    "v_mul_f32 v40, v40, v3\n"
    "v_accvgpr_read_b32 v8, a8\n"
    "v_mul_f32 v8, v8, v4\n"
    "v_accvgpr_write_b32 a8, v8\n"
    "v_accvgpr_read_b32 v9, a9\n"
    "v_mul_f32 v9, v9, v5\n"
    "v_accvgpr_write_b32 a9, v9\n"
    "v_accvgpr_read_b32 v8, a10\n"
    "v_mul_f32 v8, v8, v6\n"
    "v_accvgpr_write_b32 a10, v8\n"
    "v_accvgpr_read_b32 v9, a11\n"
    "v_mul_f32 v9, v9, v7\n"
    "v_accvgpr_write_b32 a11, v9\n"
    "v_accvgpr_read_b32 v8, a24\n"
    "v_mul_f32 v8, v8, v4\n"
    "v_accvgpr_write_b32 a24, v8\n"
    "v_accvgpr_read_b32 v9, a25\n"
    "v_mul_f32 v9, v9, v5\n"
    "v_accvgpr_write_b32 a25, v9\n"
    "v_accvgpr_read_b32 v8, a26\n"
    "v_mul_f32 v8, v8, v6\n"
    "v_accvgpr_write_b32 a26, v8\n"
    "v_accvgpr_read_b32 v9, a27\n"
    "v_mul_f32 v9, v9, v7\n"
    "v_accvgpr_write_b32 a27, v9\n"
    "v_accvgpr_read_b32 v8, a40\n"
    "v_mul_f32 v8, v8, v4\n"
    "v_accvgpr_write_b32 a40, v8\n"
    "v_accvgpr_read_b32 v9, a41\n"
    "v_mul_f32 v9, v9, v5\n"
    "v_accvgpr_write_b32 a41, v9\n"
    "v_accvgpr_read_b32 v8, a42\n"
    "v_mul_f32 v8, v8, v6\n"
    "v_accvgpr_write_b32 a42, v8\n"
    "v_accvgpr_read_b32 v9, a43\n"
    "v_mul_f32 v9, v9, v7\n"
    "v_accvgpr_write_b32 a43, v9\n"
    "v_accvgpr_read_b32 v8, a56\n"
    "v_mul_f32 v8, v8, v4\n"
    "v_accvgpr_write_b32 a56, v8\n"
    "v_accvgpr_read_b32 v9, a57\n"
    "v_mul_f32 v9, v9, v5\n"
    "v_accvgpr_write_b32 a57, v9\n"
    "v_accvgpr_read_b32 v8, a58\n"
    "v_mul_f32 v8, v8, v6\n"
    "v_accvgpr_write_b32 a58, v8\n"
    "v_accvgpr_read_b32 v9, a59\n"
    "v_mul_f32 v9, v9, v7\n"
    "v_accvgpr_write_b32 a59, v9\n"
    "v_accvgpr_read_b32 v8, a72\n"
    "v_mul_f32 v8, v8, v4\n"
    "v_accvgpr_write_b32 a72, v8\n"
    "v_accvgpr_read_b32 v9, a73\n"
    "v_mul_f32 v9, v9, v5\n"
    "v_accvgpr_write_b32 a73, v9\n"
    "v_accvgpr_read_b32 v8, a74\n"
    "v_mul_f32 v8, v8, v6\n"
    "v_accvgpr_write_b32 a74, v8\n"
    "v_accvgpr_read_b32 v9, a75\n"
    "v_mul_f32 v9, v9, v7\n"
    "v_accvgpr_write_b32 a75, v9\n"
    "v_accvgpr_read_b32 v8, a88\n"
    "v_mul_f32 v8, v8, v4\n"
    "v_accvgpr_write_b32 a88, v8\n"
    "v_accvgpr_read_b32 v9, a89\n"
    "v_mul_f32 v9, v9, v5\n"
    "v_accvgpr_write_b32 a89, v9\n"
    "v_accvgpr_read_b32 v8, a90\n"
    "v_mul_f32 v8, v8, v6\n"
    "v_accvgpr_write_b32 a90, v8\n"
    "v_accvgpr_read_b32 v9, a91\n"
    "v_mul_f32 v9, v9, v7\n"
    "v_accvgpr_write_b32 a91, v9\n"
    "v_accvgpr_read_b32 v8, a104\n"
    "v_mul_f32 v8, v8, v4\n"
    "v_accvgpr_write_b32 a104, v8\n"
    "v_accvgpr_read_b32 v9, a105\n"
    "v_mul_f32 v9, v9, v5\n"
    "v_accvgpr_write_b32 a105, v9\n"
    "v_accvgpr_read_b32 v8, a106\n"
    "v_mul_f32 v8, v8, v6\n"
    "v_accvgpr_write_b32 a106, v8\n"
    "v_accvgpr_read_b32 v9, a107\n"
    "v_mul_f32 v9, v9, v7\n"
    "v_accvgpr_write_b32 a107, v9\n"
    "v_accvgpr_read_b32 v8, a120\n"
    "v_mul_f32 v8, v8, v4\n"
    "v_accvgpr_write_b32 a120, v8\n"
    "v_accvgpr_read_b32 v9, a121\n"
    "v_mul_f32 v9, v9, v5\n"
    "v_accvgpr_write_b32 a121, v9\n"
    "v_accvgpr_read_b32 v8, a122\n"
    "v_mul_f32 v8, v8, v6\n"
    "v_accvgpr_write_b32 a122, v8\n"
    "v_accvgpr_read_b32 v9, a123\n"
    "v_mul_f32 v9, v9, v7\n"
    "v_accvgpr_write_b32 a123, v9\n"
    "v_max3_f32 v18, v76, v77, s91\n"
    "v_max3_f32 v18, v18, v78, v79\n"
    "v_max3_f32 v18, v18, v92, v93\n"
    "v_max3_f32 v18, v18, v94, v95\n"
    "v_mov_b32 v3, v18\n"
    "s_nop 1\n"
    "v_permlane32_swap_b32 v18, v3\n"
    "s_nop 1\n"
    "v_max_f32 v18, v18, v3\n"
    "v_mov_b32 v3, v18\n"
    "s_nop 1\n"
    "v_permlane16_swap_b32 v18, v3\n"
    "s_nop 1\n"
    "v_max_f32 v18, v18, v3\n"
    "v_add_f32 v18, 0x41f00000, v18\n"
    "v_ceil_f32 v18, v18\n"
    "v_and_b32 v4, 15, v25\n"
    "v_lshl_add_u32 v4, v4, 2, s86\n"
    "ds_read_b32 v5, v4\n"
    "s_waitcnt lgkmcnt(0)\n"
    "v_sub_f32 v5, v5, v18\n"
    "ds_write_b32 v4, v5\n"
    "v_sub_f32 v76, v76, v18\n"
    "v_sub_f32 v77, v77, v18\n"
    "v_sub_f32 v78, v78, v18\n"
    "v_sub_f32 v79, v79, v18\n"
    "v_sub_f32 v92, v92, v18\n"
    "v_sub_f32 v93, v93, v18\n"
    "v_sub_f32 v94, v94, v18\n"
    "v_sub_f32 v95, v95, v18\n"
    "v_sub_f32 v108, v108, v18\n"
    "v_sub_f32 v109, v109, v18\n"
    "v_sub_f32 v110, v110, v18\n"
    "v_sub_f32 v111, v111, v18\n"
    "v_sub_f32 v124, v124, v18\n"
    "v_sub_f32 v125, v125, v18\n"
    "v_sub_f32 v126, v126, v18\n"
    "v_sub_f32 v127, v127, v18\n"
    "v_max_f32 v18, 0, v18\n"
    "v_exp_f32 v3, -v18\n"
    "s_waitcnt lgkmcnt(0)\n"
    "ds_bpermute_b32 v4, v2, v3 offset:0\n"
    "ds_bpermute_b32 v5, v2, v3 offset:4\n"
    "ds_bpermute_b32 v6, v2, v3 offset:8\n"
    "ds_bpermute_b32 v7, v2, v3 offset:12\n"
    "s_waitcnt lgkmcnt(0)\n"
    "v_mul_f32 v44, v44, v3\n"
    "v_mul_f32 v160, v160, v4\n"
    "v_mul_f32 v161, v161, v5\n"
    "v_mul_f32 v162, v162, v6\n"
    "v_mul_f32 v163, v163, v7\n"
    "v_mul_f32 v164, v164, v4\n"
    "v_mul_f32 v165, v165, v5\n"
    "v_mul_f32 v166, v166, v6\n"
    "v_mul_f32 v167, v167, v7\n"
    "v_mul_f32 v168, v168, v4\n"
    "v_mul_f32 v169, v169, v5\n"
    "v_mul_f32 v170, v170, v6\n"
    "v_mul_f32 v171, v171, v7\n"
    "v_mul_f32 v172, v172, v4\n"
    "v_mul_f32 v173, v173, v5\n"
    "v_mul_f32 v174, v174, v6\n"
    "v_mul_f32 v175, v175, v7\n"
    "v_mul_f32 v176, v176, v4\n"
    "v_mul_f32 v177, v177, v5\n"
    "v_mul_f32 v178, v178, v6\n"
    "v_mul_f32 v179, v179, v7\n"
    "v_mul_f32 v180, v180, v4\n"
    "v_mul_f32 v181, v181, v5\n"
    "v_mul_f32 v182, v182, v6\n"
    "v_mul_f32 v183, v183, v7\n"
    "v_mul_f32 v184, v184, v4\n"
    "v_mul_f32 v185, v185, v5\n"
    "v_mul_f32 v186, v186, v6\n"
    "v_mul_f32 v187, v187, v7\n"
    "v_mul_f32 v188, v188, v4\n"
    "v_mul_f32 v189, v189, v5\n"
    "v_mul_f32 v190, v190, v6\n"
    "v_mul_f32 v191, v191, v7\n"
    "v_exp_f32 v2, v64\n"
    "v_exp_f32 v3, v65\n"
    "v_exp_f32 v4, v66\n"
    "v_exp_f32 v5, v67\n"
    "v_cvt_pk_bf16_f32 v128, v2, v3\n"
    "v_exp_f32 v6, v80\n"
    "v_exp_f32 v7, v81\n"
    "v_cvt_pk_bf16_f32 v129, v4, v5\n"
    "v_exp_f32 v2, v82\n"
    "v_exp_f32 v3, v83\n"
    "v_cvt_pk_bf16_f32 v130, v6, v7\n"
    "v_exp_f32 v4, v68\n"
    "v_exp_f32 v5, v69\n"
    "v_cvt_pk_bf16_f32 v131, v2, v3\n"
    "v_exp_f32 v6, v70\n"
    "v_exp_f32 v7, v71\n"
    "v_cvt_pk_bf16_f32 v132, v4, v5\n"
    "v_exp_f32 v2, v84\n"
    "v_exp_f32 v3, v85\n"
    "v_cvt_pk_bf16_f32 v133, v6, v7\n"
    "v_exp_f32 v4, v86\n"
    "v_exp_f32 v5, v87\n"
    "v_cvt_pk_bf16_f32 v134, v2, v3\n"
    "v_exp_f32 v6, v72\n"
    "v_exp_f32 v7, v73\n"
    "v_cvt_pk_bf16_f32 v135, v4, v5\n"
    "v_exp_f32 v2, v74\n"
    "v_exp_f32 v3, v75\n"
    "v_cvt_pk_bf16_f32 v136, v6, v7\n"
    "v_exp_f32 v4, v88\n"
    "v_exp_f32 v5, v89\n"
    "v_cvt_pk_bf16_f32 v137, v2, v3\n"
    "v_exp_f32 v6, v90\n"
    "v_exp_f32 v7, v91\n"
    "v_cvt_pk_bf16_f32 v138, v4, v5\n"
    "v_exp_f32 v2, v76\n"
    "v_exp_f32 v3, v77\n"
    "v_cvt_pk_bf16_f32 v139, v6, v7\n"
    "v_exp_f32 v4, v78\n"
    "v_exp_f32 v5, v79\n"
    "v_cvt_pk_bf16_f32 v140, v2, v3\n"
    "v_exp_f32 v6, v92\n"
    "v_exp_f32 v7, v93\n"
    "v_cvt_pk_bf16_f32 v141, v4, v5\n"
    "v_exp_f32 v2, v94\n"
    "v_exp_f32 v3, v95\n"
    "v_cvt_pk_bf16_f32 v142, v6, v7\n"
    "v_cvt_pk_bf16_f32 v143, v2, v3\n"
    "v_lshrrev_b32 v9, 4, v25\n"
    "v_mad_u32_u24 v9, v9, 48, s64\n"
    "s_nop 15\n"
    "s_mov_b64 vcc, -1\n"
    "s_setpc_b64 s[76:77]\n"
    ".Lf_ADDM1:\n"
    "s_nop 15\n"
    "v_and_b32 v4, 15, v25\n"
    "v_lshl_add_u32 v4, v4, 2, s68\n"
    "ds_read_b32 v5, v4\n"
    "s_waitcnt lgkmcnt(0)\n"
    "v_add_f32 v64, v64, v5\n"
    "v_add_f32 v65, v65, v5\n"
    "v_add_f32 v66, v66, v5\n"
    "v_add_f32 v67, v67, v5\n"
    "v_add_f32 v80, v80, v5\n"
    "v_add_f32 v81, v81, v5\n"
    "v_add_f32 v82, v82, v5\n"
    "v_add_f32 v83, v83, v5\n"
    "v_and_b32 v4, 15, v25\n"
    "v_lshl_add_u32 v4, v4, 2, s70\n"
    "ds_read_b32 v5, v4\n"
    "s_waitcnt lgkmcnt(0)\n"
    "v_add_f32 v68, v68, v5\n"
    "v_add_f32 v69, v69, v5\n"
    "v_add_f32 v70, v70, v5\n"
    "v_add_f32 v71, v71, v5\n"
    "v_add_f32 v84, v84, v5\n"
    "v_add_f32 v85, v85, v5\n"
    "v_add_f32 v86, v86, v5\n"
    "v_add_f32 v87, v87, v5\n"
    "v_and_b32 v4, 15, v25\n"
    "v_lshl_add_u32 v4, v4, 2, s71\n"
    "ds_read_b32 v5, v4\n"
    "s_waitcnt lgkmcnt(0)\n"
    "v_add_f32 v72, v72, v5\n"
    "v_add_f32 v73, v73, v5\n"
    "v_add_f32 v74, v74, v5\n"
    "v_add_f32 v75, v75, v5\n"
    "v_add_f32 v88, v88, v5\n"
    "v_add_f32 v89, v89, v5\n"
    "v_add_f32 v90, v90, v5\n"
    "v_add_f32 v91, v91, v5\n"
    "v_and_b32 v4, 15, v25\n"
    "v_lshl_add_u32 v4, v4, 2, s86\n"
    "ds_read_b32 v5, v4\n"
    "s_waitcnt lgkmcnt(0)\n"
    "v_add_f32 v76, v76, v5\n"
    "v_add_f32 v77, v77, v5\n"
    "v_add_f32 v78, v78, v5\n"
    "v_add_f32 v79, v79, v5\n"
    "v_add_f32 v92, v92, v5\n"
    "v_add_f32 v93, v93, v5\n"
    "v_add_f32 v94, v94, v5\n"
    "v_add_f32 v95, v95, v5\n"
    "v_or3_b32 v18, v144, v145, 0\n"
    "v_or3_b32 v18, v146, v147, v18\n"
    "v_or3_b32 v18, v148, v149, v18\n"
    "v_or3_b32 v18, v150, v151, v18\n"
    "v_or3_b32 v18, v152, v153, v18\n"
    "v_or3_b32 v18, v154, v155, v18\n"
    "v_or3_b32 v18, v156, v157, v18\n"
    "v_or3_b32 v18, v158, v159, v18\n"
    "v_and_b32 v18, 0x40004000, v18\n"
    "v_cmp_ne_u32 vcc, 0, v18\n"
    "s_cbranch_vccnz .Lf_RARE1\n"
    "s_mov_b64 vcc, -1\n"
    "s_setpc_b64 s[76:77]\n"
    ".Lf_RARE1:\n"
    "s_nop 15\n"
    "s_mov_b32 s73, 1\n"
    "v_mov_b32 v2, 126992\n"
    "ds_write_b32 v2, v2\n"
    "s_waitcnt lgkmcnt(0)\n"
    "v_lshrrev_b32 v2, 4, v25\n"
    "v_lshlrev_b32 v2, 4, v2\n"
    "v_max3_f32 v18, v96, v97, s91\n"
    "v_max3_f32 v18, v18, v98, v99\n"
    "v_max3_f32 v18, v18, v112, v113\n"
    "v_max3_f32 v18, v18, v114, v115\n"
    "v_mov_b32 v3, v18\n"
    "s_nop 1\n"
    "v_permlane32_swap_b32 v18, v3\n"
    "s_nop 1\n"
    "v_max_f32 v18, v18, v3\n"
    "v_mov_b32 v3, v18\n"
    "s_nop 1\n"
    "v_permlane16_swap_b32 v18, v3\n"
    "s_nop 1\n"
    "v_max_f32 v18, v18, v3\n"
    "v_add_f32 v18, 0x41f00000, v18\n"
    "v_ceil_f32 v18, v18\n"
    "v_and_b32 v4, 15, v25\n"
    "v_lshl_add_u32 v4, v4, 2, s68\n"
    "ds_read_b32 v5, v4\n"
    "s_waitcnt lgkmcnt(0)\n"
    "v_sub_f32 v5, v5, v18\n"
    "ds_write_b32 v4, v5\n"
    "v_sub_f32 v96, v96, v18\n"
    "v_sub_f32 v97, v97, v18\n"
    "v_sub_f32 v98, v98, v18\n"
    "v_sub_f32 v99, v99, v18\n"
    "v_sub_f32 v112, v112, v18\n"
    "v_sub_f32 v113, v113, v18\n"
    "v_sub_f32 v114, v114, v18\n"
    "v_sub_f32 v115, v115, v18\n"
    "v_sub_f32 v64, v64, v18\n"
    "v_sub_f32 v65, v65, v18\n"
    "v_sub_f32 v66, v66, v18\n"
    "v_sub_f32 v67, v67, v18\n"
    "v_sub_f32 v80, v80, v18\n"
    "v_sub_f32 v81, v81, v18\n"
    "v_sub_f32 v82, v82, v18\n"
    "v_sub_f32 v83, v83, v18\n"
    "v_max_f32 v18, 0, v18\n"
    "v_exp_f32 v3, -v18\n"
    "s_waitcnt lgkmcnt(0)\n"
    "ds_bpermute_b32 v4, v2, v3 offset:0\n"
    "ds_bpermute_b32 v5, v2, v3 offset:4\n"
    "ds_bpermute_b32 v6, v2, v3 offset:8\n"
    "ds_bpermute_b32 v7, v2, v3 offset:12\n"
    "s_waitcnt lgkmcnt(0)\n"
    "v_mul_f32 v10, v10, v3\n"
    "v_accvgpr_read_b32 v8, a0\n"
    "v_mul_f32 v8, v8, v4\n"
    "v_accvgpr_write_b32 a0, v8\n"
    "v_accvgpr_read_b32 v9, a1\n"
    "v_mul_f32 v9, v9, v5\n"
    "v_accvgpr_write_b32 a1, v9\n"
    "v_accvgpr_read_b32 v8, a2\n"
    "v_mul_f32 v8, v8, v6\n"
    "v_accvgpr_write_b32 a2, v8\n"
    "v_accvgpr_read_b32 v9, a3\n"
    "v_mul_f32 v9, v9, v7\n"
    "v_accvgpr_write_b32 a3, v9\n"
    "v_accvgpr_read_b32 v8, a16\n"
    "v_mul_f32 v8, v8, v4\n"
    "v_accvgpr_write_b32 a16, v8\n"
    "v_accvgpr_read_b32 v9, a17\n"
    "v_mul_f32 v9, v9, v5\n"
    "v_accvgpr_write_b32 a17, v9\n"
    "v_accvgpr_read_b32 v8, a18\n"
    "v_mul_f32 v8, v8, v6\n"
    "v_accvgpr_write_b32 a18, v8\n"
    "v_accvgpr_read_b32 v9, a19\n"
    "v_mul_f32 v9, v9, v7\n"
    "v_accvgpr_write_b32 a19, v9\n"
    "v_accvgpr_read_b32 v8, a32\n"
    "v_mul_f32 v8, v8, v4\n"
    "v_accvgpr_write_b32 a32, v8\n"
    "v_accvgpr_read_b32 v9, a33\n"
    "v_mul_f32 v9, v9, v5\n"
    "v_accvgpr_write_b32 a33, v9\n"
    "v_accvgpr_read_b32 v8, a34\n"
    "v_mul_f32 v8, v8, v6\n"
    "v_accvgpr_write_b32 a34, v8\n"
    "v_accvgpr_read_b32 v9, a35\n"
    "v_mul_f32 v9, v9, v7\n"
    "v_accvgpr_write_b32 a35, v9\n"
    "v_accvgpr_read_b32 v8, a48\n"
    "v_mul_f32 v8, v8, v4\n"
    "v_accvgpr_write_b32 a48, v8\n"
    "v_accvgpr_read_b32 v9, a49\n"
    "v_mul_f32 v9, v9, v5\n"
    "v_accvgpr_write_b32 a49, v9\n"
    "v_accvgpr_read_b32 v8, a50\n"
    "v_mul_f32 v8, v8, v6\n"
    "v_accvgpr_write_b32 a50, v8\n"
    "v_accvgpr_read_b32 v9, a51\n"
    "v_mul_f32 v9, v9, v7\n"
    "v_accvgpr_write_b32 a51, v9\n"
    "v_accvgpr_read_b32 v8, a64\n"
    "v_mul_f32 v8, v8, v4\n"
    "v_accvgpr_write_b32 a64, v8\n"
    "v_accvgpr_read_b32 v9, a65\n"
    "v_mul_f32 v9, v9, v5\n"
    "v_accvgpr_write_b32 a65, v9\n"
    "v_accvgpr_read_b32 v8, a66\n"
    "v_mul_f32 v8, v8, v6\n"
    "v_accvgpr_write_b32 a66, v8\n"
    "v_accvgpr_read_b32 v9, a67\n"
    "v_mul_f32 v9, v9, v7\n"
    "v_accvgpr_write_b32 a67, v9\n"
    "v_accvgpr_read_b32 v8, a80\n"
    "v_mul_f32 v8, v8, v4\n"
    "v_accvgpr_write_b32 a80, v8\n"
    "v_accvgpr_read_b32 v9, a81\n"
    "v_mul_f32 v9, v9, v5\n"
    "v_accvgpr_write_b32 a81, v9\n"
    "v_accvgpr_read_b32 v8, a82\n"
    "v_mul_f32 v8, v8, v6\n"
    "v_accvgpr_write_b32 a82, v8\n"
    "v_accvgpr_read_b32 v9, a83\n"
    "v_mul_f32 v9, v9, v7\n"
    "v_accvgpr_write_b32 a83, v9\n"
    "v_accvgpr_read_b32 v8, a96\n"
    "v_mul_f32 v8, v8, v4\n"
    "v_accvgpr_write_b32 a96, v8\n"
    "v_accvgpr_read_b32 v9, a97\n"
    "v_mul_f32 v9, v9, v5\n"
    "v_accvgpr_write_b32 a97, v9\n"
    "v_accvgpr_read_b32 v8, a98\n"
    "v_mul_f32 v8, v8, v6\n"
    "v_accvgpr_write_b32 a98, v8\n"
    "v_accvgpr_read_b32 v9, a99\n"
    "v_mul_f32 v9, v9, v7\n"
    "v_accvgpr_write_b32 a99, v9\n"
    "v_accvgpr_read_b32 v8, a112\n"
    "v_mul_f32 v8, v8, v4\n"
    "v_accvgpr_write_b32 a112, v8\n"
    "v_accvgpr_read_b32 v9, a113\n"
    "v_mul_f32 v9, v9, v5\n"
    "v_accvgpr_write_b32 a113, v9\n"
    "v_accvgpr_read_b32 v8, a114\n"
    "v_mul_f32 v8, v8, v6\n"
    "v_accvgpr_write_b32 a114, v8\n"
    "v_accvgpr_read_b32 v9, a115\n"
    "v_mul_f32 v9, v9, v7\n"
    "v_accvgpr_write_b32 a115, v9\n"
    "v_max3_f32 v18, v100, v101, s91\n"
    "v_max3_f32 v18, v18, v102, v103\n"
    "v_max3_f32 v18, v18, v116, v117\n"
    "v_max3_f32 v18, v18, v118, v119\n"
    "v_mov_b32 v3, v18\n"
    "s_nop 1\n"
    "v_permlane32_swap_b32 v18, v3\n"
    "s_nop 1\n"
    "v_max_f32 v18, v18, v3\n"
    "v_mov_b32 v3, v18\n"
    "s_nop 1\n"
    "v_permlane16_swap_b32 v18, v3\n"
    "s_nop 1\n"
    "v_max_f32 v18, v18, v3\n"
    "v_add_f32 v18, 0x41f00000, v18\n"
    "v_ceil_f32 v18, v18\n"
    "v_and_b32 v4, 15, v25\n"
    "v_lshl_add_u32 v4, v4, 2, s70\n"
    "ds_read_b32 v5, v4\n"
    "s_waitcnt lgkmcnt(0)\n"
    "v_sub_f32 v5, v5, v18\n"
    "ds_write_b32 v4, v5\n"
    "v_sub_f32 v100, v100, v18\n"
    "v_sub_f32 v101, v101, v18\n"
    "v_sub_f32 v102, v102, v18\n"
    "v_sub_f32 v103, v103, v18\n"
    "v_sub_f32 v116, v116, v18\n"
    "v_sub_f32 v117, v117, v18\n"
    "v_sub_f32 v118, v118, v18\n"
    "v_sub_f32 v119, v119, v18\n"
    "v_sub_f32 v68, v68, v18\n"
    "v_sub_f32 v69, v69, v18\n"
    "v_sub_f32 v70, v70, v18\n"
    "v_sub_f32 v71, v71, v18\n"
    "v_sub_f32 v84, v84, v18\n"
    "v_sub_f32 v85, v85, v18\n"
    "v_sub_f32 v86, v86, v18\n"
    "v_sub_f32 v87, v87, v18\n"
    "v_max_f32 v18, 0, v18\n"
    "v_exp_f32 v3, -v18\n"
    "s_waitcnt lgkmcnt(0)\n"
    "ds_bpermute_b32 v4, v2, v3 offset:0\n"
    "ds_bpermute_b32 v5, v2, v3 offset:4\n"
    "ds_bpermute_b32 v6, v2, v3 offset:8\n"
    "ds_bpermute_b32 v7, v2, v3 offset:12\n"
    "s_waitcnt lgkmcnt(0)\n"
    "v_mul_f32 v14, v14, v3\n"
    "v_accvgpr_read_b32 v8, a4\n"
    "v_mul_f32 v8, v8, v4\n"
    "v_accvgpr_write_b32 a4, v8\n"
    "v_accvgpr_read_b32 v9, a5\n"
    "v_mul_f32 v9, v9, v5\n"
    "v_accvgpr_write_b32 a5, v9\n"
    "v_accvgpr_read_b32 v8, a6\n"
    "v_mul_f32 v8, v8, v6\n"
    "v_accvgpr_write_b32 a6, v8\n"
    "v_accvgpr_read_b32 v9, a7\n"
    "v_mul_f32 v9, v9, v7\n"
    "v_accvgpr_write_b32 a7, v9\n"
    "v_accvgpr_read_b32 v8, a20\n"
    "v_mul_f32 v8, v8, v4\n"
    "v_accvgpr_write_b32 a20, v8\n"
    "v_accvgpr_read_b32 v9, a21\n"
    "v_mul_f32 v9, v9, v5\n"
    "v_accvgpr_write_b32 a21, v9\n"
    "v_accvgpr_read_b32 v8, a22\n"
    "v_mul_f32 v8, v8, v6\n"
    "v_accvgpr_write_b32 a22, v8\n"
    "v_accvgpr_read_b32 v9, a23\n"
    "v_mul_f32 v9, v9, v7\n"
    "v_accvgpr_write_b32 a23, v9\n"
    "v_accvgpr_read_b32 v8, a36\n"
    "v_mul_f32 v8, v8, v4\n"
    "v_accvgpr_write_b32 a36, v8\n"
    "v_accvgpr_read_b32 v9, a37\n"
    "v_mul_f32 v9, v9, v5\n"
    "v_accvgpr_write_b32 a37, v9\n"
    "v_accvgpr_read_b32 v8, a38\n"
    "v_mul_f32 v8, v8, v6\n"
    "v_accvgpr_write_b32 a38, v8\n"
    "v_accvgpr_read_b32 v9, a39\n"
    "v_mul_f32 v9, v9, v7\n"
    "v_accvgpr_write_b32 a39, v9\n"
    "v_accvgpr_read_b32 v8, a52\n"
    "v_mul_f32 v8, v8, v4\n"
    "v_accvgpr_write_b32 a52, v8\n"
    "v_accvgpr_read_b32 v9, a53\n"
    "v_mul_f32 v9, v9, v5\n"
    "v_accvgpr_write_b32 a53, v9\n"
    "v_accvgpr_read_b32 v8, a54\n"
    "v_mul_f32 v8, v8, v6\n"
    "v_accvgpr_write_b32 a54, v8\n"
    "v_accvgpr_read_b32 v9, a55\n"
    "v_mul_f32 v9, v9, v7\n"
    "v_accvgpr_write_b32 a55, v9\n"
    "v_accvgpr_read_b32 v8, a68\n"
    "v_mul_f32 v8, v8, v4\n"
    "v_accvgpr_write_b32 a68, v8\n"
    "v_accvgpr_read_b32 v9, a69\n"
    "v_mul_f32 v9, v9, v5\n"
    "v_accvgpr_write_b32 a69, v9\n"
    "v_accvgpr_read_b32 v8, a70\n"
    "v_mul_f32 v8, v8, v6\n"
    "v_accvgpr_write_b32 a70, v8\n"
    "v_accvgpr_read_b32 v9, a71\n"
    "v_mul_f32 v9, v9, v7\n"
    "v_accvgpr_write_b32 a71, v9\n"
    "v_accvgpr_read_b32 v8, a84\n"
    "v_mul_f32 v8, v8, v4\n"
    "v_accvgpr_write_b32 a84, v8\n"
    "v_accvgpr_read_b32 v9, a85\n"
    "v_mul_f32 v9, v9, v5\n"
    "v_accvgpr_write_b32 a85, v9\n"
    "v_accvgpr_read_b32 v8, a86\n"
    "v_mul_f32 v8, v8, v6\n"
    "v_accvgpr_write_b32 a86, v8\n"
    "v_accvgpr_read_b32 v9, a87\n"
    "v_mul_f32 v9, v9, v7\n"
    "v_accvgpr_write_b32 a87, v9\n"
    "v_accvgpr_read_b32 v8, a100\n"
    "v_mul_f32 v8, v8, v4\n"
    "v_accvgpr_write_b32 a100, v8\n"
    "v_accvgpr_read_b32 v9, a101\n"
    "v_mul_f32 v9, v9, v5\n"
    "v_accvgpr_write_b32 a101, v9\n"
    "v_accvgpr_read_b32 v8, a102\n"
    "v_mul_f32 v8, v8, v6\n"
    "v_accvgpr_write_b32 a102, v8\n"
    "v_accvgpr_read_b32 v9, a103\n"
    "v_mul_f32 v9, v9, v7\n"
    "v_accvgpr_write_b32 a103, v9\n"
    "v_accvgpr_read_b32 v8, a116\n"
    "v_mul_f32 v8, v8, v4\n"
    "v_accvgpr_write_b32 a116, v8\n"
    "v_accvgpr_read_b32 v9, a117\n"
    "v_mul_f32 v9, v9, v5\n"
    "v_accvgpr_write_b32 a117, v9\n"
    "v_accvgpr_read_b32 v8, a118\n"
    "v_mul_f32 v8, v8, v6\n"
    "v_accvgpr_write_b32 a118, v8\n"
    "v_accvgpr_read_b32 v9, a119\n"
    "v_mul_f32 v9, v9, v7\n"
    "v_accvgpr_write_b32 a119, v9\n"
    "v_max3_f32 v18, v104, v105, s91\n"
    "v_max3_f32 v18, v18, v106, v107\n"
    "v_max3_f32 v18, v18, v120, v121\n"
    "v_max3_f32 v18, v18, v122, v123\n"
    "v_mov_b32 v3, v18\n"
    "s_nop 1\n"
    "v_permlane32_swap_b32 v18, v3\n"
    "s_nop 1\n"
    "v_max_f32 v18, v18, v3\n"
    "v_mov_b32 v3, v18\n"
    "s_nop 1\n"
    "v_permlane16_swap_b32 v18, v3\n"
    "s_nop 1\n"
    "v_max_f32 v18, v18, v3\n"
    "v_add_f32 v18, 0x41f00000, v18\n"
    "v_ceil_f32 v18, v18\n"
    "v_and_b32 v4, 15, v25\n"
    "v_lshl_add_u32 v4, v4, 2, s71\n"
    "ds_read_b32 v5, v4\n"
    "s_waitcnt lgkmcnt(0)\n"
    "v_sub_f32 v5, v5, v18\n"
    "ds_write_b32 v4, v5\n"
    "v_sub_f32 v104, v104, v18\n"
    "v_sub_f32 v105, v105, v18\n"
    "v_sub_f32 v106, v106, v18\n"
    "v_sub_f32 v107, v107, v18\n"
    "v_sub_f32 v120, v120, v18\n"
    "v_sub_f32 v121, v121, v18\n"
    "v_sub_f32 v122, v122, v18\n"
    "v_sub_f32 v123, v123, v18\n"
    "v_sub_f32 v72, v72, v18\n"
    "v_sub_f32 v73, v73, v18\n"
    "v_sub_f32 v74, v74, v18\n"
    "v_sub_f32 v75, v75, v18\n"
    "v_sub_f32 v88, v88, v18\n"
    "v_sub_f32 v89, v89, v18\n"
    "v_sub_f32 v90, v90, v18\n"
    "v_sub_f32 v91, v91, v18\n"
    "v_max_f32 v18, 0, v18\n"
    "v_exp_f32 v3, -v18\n"
    "s_waitcnt lgkmcnt(0)\n"
    "ds_bpermute_b32 v4, v2, v3 offset:0\n"
    "ds_bpermute_b32 v5, v2, v3 offset:4\n"
    "ds_bpermute_b32 v6, v2, v3 offset:8\n"
    "ds_bpermute_b32 v7, v2, v3 offset:12\n"
    "s_waitcnt lgkmcnt(0)\n"
    "v_mul_f32 v40, v40, v3\n"
    "v_accvgpr_read_b32 v8, a8\n"
    "v_mul_f32 v8, v8, v4\n"
    "v_accvgpr_write_b32 a8, v8\n"
    "v_accvgpr_read_b32 v9, a9\n"
    "v_mul_f32 v9, v9, v5\n"
    "v_accvgpr_write_b32 a9, v9\n"
    "v_accvgpr_read_b32 v8, a10\n"
    "v_mul_f32 v8, v8, v6\n"
    "v_accvgpr_write_b32 a10, v8\n"
    "v_accvgpr_read_b32 v9, a11\n"
    "v_mul_f32 v9, v9, v7\n"
    "v_accvgpr_write_b32 a11, v9\n"
    "v_accvgpr_read_b32 v8, a24\n"
    "v_mul_f32 v8, v8, v4\n"
    "v_accvgpr_write_b32 a24, v8\n"
    "v_accvgpr_read_b32 v9, a25\n"
    "v_mul_f32 v9, v9, v5\n"
    "v_accvgpr_write_b32 a25, v9\n"
    "v_accvgpr_read_b32 v8, a26\n"
    "v_mul_f32 v8, v8, v6\n"
    "v_accvgpr_write_b32 a26, v8\n"
    "v_accvgpr_read_b32 v9, a27\n"
    "v_mul_f32 v9, v9, v7\n"
    "v_accvgpr_write_b32 a27, v9\n"
    "v_accvgpr_read_b32 v8, a40\n"
    "v_mul_f32 v8, v8, v4\n"
    "v_accvgpr_write_b32 a40, v8\n"
    "v_accvgpr_read_b32 v9, a41\n"
    "v_mul_f32 v9, v9, v5\n"
    "v_accvgpr_write_b32 a41, v9\n"
    "v_accvgpr_read_b32 v8, a42\n"
    "v_mul_f32 v8, v8, v6\n"
    "v_accvgpr_write_b32 a42, v8\n"
    "v_accvgpr_read_b32 v9, a43\n"
    "v_mul_f32 v9, v9, v7\n"
    "v_accvgpr_write_b32 a43, v9\n"
    "v_accvgpr_read_b32 v8, a56\n"
    "v_mul_f32 v8, v8, v4\n"
    "v_accvgpr_write_b32 a56, v8\n"
    "v_accvgpr_read_b32 v9, a57\n"
    "v_mul_f32 v9, v9, v5\n"
    "v_accvgpr_write_b32 a57, v9\n"
    "v_accvgpr_read_b32 v8, a58\n"
    "v_mul_f32 v8, v8, v6\n"
    "v_accvgpr_write_b32 a58, v8\n"
    "v_accvgpr_read_b32 v9, a59\n"
    "v_mul_f32 v9, v9, v7\n"
    "v_accvgpr_write_b32 a59, v9\n"
    "v_accvgpr_read_b32 v8, a72\n"
    "v_mul_f32 v8, v8, v4\n"
    "v_accvgpr_write_b32 a72, v8\n"
    "v_accvgpr_read_b32 v9, a73\n"
    "v_mul_f32 v9, v9, v5\n"
    "v_accvgpr_write_b32 a73, v9\n"
    "v_accvgpr_read_b32 v8, a74\n"
    "v_mul_f32 v8, v8, v6\n"
    "v_accvgpr_write_b32 a74, v8\n"
    "v_accvgpr_read_b32 v9, a75\n"
    "v_mul_f32 v9, v9, v7\n"
    "v_accvgpr_write_b32 a75, v9\n"
    "v_accvgpr_read_b32 v8, a88\n"
    "v_mul_f32 v8, v8, v4\n"
    "v_accvgpr_write_b32 a88, v8\n"
    "v_accvgpr_read_b32 v9, a89\n"
    "v_mul_f32 v9, v9, v5\n"
    "v_accvgpr_write_b32 a89, v9\n"
    "v_accvgpr_read_b32 v8, a90\n"
    "v_mul_f32 v8, v8, v6\n"
    "v_accvgpr_write_b32 a90, v8\n"
    "v_accvgpr_read_b32 v9, a91\n"
    "v_mul_f32 v9, v9, v7\n"
    "v_accvgpr_write_b32 a91, v9\n"
    "v_accvgpr_read_b32 v8, a104\n"
    "v_mul_f32 v8, v8, v4\n"
    "v_accvgpr_write_b32 a104, v8\n"
    "v_accvgpr_read_b32 v9, a105\n"
    "v_mul_f32 v9, v9, v5\n"
    "v_accvgpr_write_b32 a105, v9\n"
    "v_accvgpr_read_b32 v8, a106\n"
    "v_mul_f32 v8, v8, v6\n"
    "v_accvgpr_write_b32 a106, v8\n"
    "v_accvgpr_read_b32 v9, a107\n"
    "v_mul_f32 v9, v9, v7\n"
    "v_accvgpr_write_b32 a107, v9\n"
    "v_accvgpr_read_b32 v8, a120\n"
    "v_mul_f32 v8, v8, v4\n"
    "v_accvgpr_write_b32 a120, v8\n"
    "v_accvgpr_read_b32 v9, a121\n"
    "v_mul_f32 v9, v9, v5\n"
    "v_accvgpr_write_b32 a121, v9\n"
    "v_accvgpr_read_b32 v8, a122\n"
    "v_mul_f32 v8, v8, v6\n"
    "v_accvgpr_write_b32 a122, v8\n"
    "v_accvgpr_read_b32 v9, a123\n"
    "v_mul_f32 v9, v9, v7\n"
    "v_accvgpr_write_b32 a123, v9\n"
    "v_max3_f32 v18, v108, v109, s91\n"
    "v_max3_f32 v18, v18, v110, v111\n"
    "v_max3_f32 v18, v18, v124, v125\n"
    "v_max3_f32 v18, v18, v126, v127\n"
    "v_mov_b32 v3, v18\n"
    "s_nop 1\n"
    "v_permlane32_swap_b32 v18, v3\n"
    "s_nop 1\n"
    "v_max_f32 v18, v18, v3\n"
    "v_mov_b32 v3, v18\n"
    "s_nop 1\n"
    "v_permlane16_swap_b32 v18, v3\n"
    "s_nop 1\n"
    "v_max_f32 v18, v18, v3\n"
    "v_add_f32 v18, 0x41f00000, v18\n"
    "v_ceil_f32 v18, v18\n"
    "v_and_b32 v4, 15, v25\n"
    "v_lshl_add_u32 v4, v4, 2, s86\n"
    "ds_read_b32 v5, v4\n"
    "s_waitcnt lgkmcnt(0)\n"
    "v_sub_f32 v5, v5, v18\n"
    "ds_write_b32 v4, v5\n"
    "v_sub_f32 v108, v108, v18\n"
    "v_sub_f32 v109, v109, v18\n"
    "v_sub_f32 v110, v110, v18\n"
    "v_sub_f32 v111, v111, v18\n"
    "v_sub_f32 v124, v124, v18\n"
    "v_sub_f32 v125, v125, v18\n"
    "v_sub_f32 v126, v126, v18\n"
    "v_sub_f32 v127, v127, v18\n"
    "v_sub_f32 v76, v76, v18\n"
    "v_sub_f32 v77, v77, v18\n"
    "v_sub_f32 v78, v78, v18\n"
    "v_sub_f32 v79, v79, v18\n"
    "v_sub_f32 v92, v92, v18\n"
    "v_sub_f32 v93, v93, v18\n"
    "v_sub_f32 v94, v94, v18\n"
    "v_sub_f32 v95, v95, v18\n"
    "v_max_f32 v18, 0, v18\n"
    "v_exp_f32 v3, -v18\n"
    "s_waitcnt lgkmcnt(0)\n"
    "ds_bpermute_b32 v4, v2, v3 offset:0\n"
    "ds_bpermute_b32 v5, v2, v3 offset:4\n"
    "ds_bpermute_b32 v6, v2, v3 offset:8\n"
    "ds_bpermute_b32 v7, v2, v3 offset:12\n"
    "s_waitcnt lgkmcnt(0)\n"
    "v_mul_f32 v44, v44, v3\n"
    "v_mul_f32 v160, v160, v4\n"
    "v_mul_f32 v161, v161, v5\n"
    "v_mul_f32 v162, v162, v6\n"
    "v_mul_f32 v163, v163, v7\n"
    "v_mul_f32 v164, v164, v4\n"
    "v_mul_f32 v165, v165, v5\n"
    "v_mul_f32 v166, v166, v6\n"
    "v_mul_f32 v167, v167, v7\n"
    "v_mul_f32 v168, v168, v4\n"
    "v_mul_f32 v169, v169, v5\n"
    "v_mul_f32 v170, v170, v6\n"
    "v_mul_f32 v171, v171, v7\n"
    "v_mul_f32 v172, v172, v4\n"
    "v_mul_f32 v173, v173, v5\n"
    "v_mul_f32 v174, v174, v6\n"
    "v_mul_f32 v175, v175, v7\n"
    "v_mul_f32 v176, v176, v4\n"
    "v_mul_f32 v177, v177, v5\n"
    "v_mul_f32 v178, v178, v6\n"
    "v_mul_f32 v179, v179, v7\n"
    "v_mul_f32 v180, v180, v4\n"
    "v_mul_f32 v181, v181, v5\n"
    "v_mul_f32 v182, v182, v6\n"
    "v_mul_f32 v183, v183, v7\n"
    "v_mul_f32 v184, v184, v4\n"
    "v_mul_f32 v185, v185, v5\n"
    "v_mul_f32 v186, v186, v6\n"
    "v_mul_f32 v187, v187, v7\n"
    "v_mul_f32 v188, v188, v4\n"
    "v_mul_f32 v189, v189, v5\n"
    "v_mul_f32 v190, v190, v6\n"
    "v_mul_f32 v191, v191, v7\n"
    "v_exp_f32 v2, v96\n"
    "v_exp_f32 v3, v97\n"
    "v_exp_f32 v4, v98\n"
    "v_exp_f32 v5, v99\n"
    "v_cvt_pk_bf16_f32 v144, v2, v3\n"
    "v_exp_f32 v6, v112\n"
    "v_exp_f32 v7, v113\n"
    "v_cvt_pk_bf16_f32 v145, v4, v5\n"
    "v_exp_f32 v2, v114\n"
    "v_exp_f32 v3, v115\n"
    "v_cvt_pk_bf16_f32 v146, v6, v7\n"
    "v_exp_f32 v4, v100\n"
    "v_exp_f32 v5, v101\n"
    "v_cvt_pk_bf16_f32 v147, v2, v3\n"
    "v_exp_f32 v6, v102\n"
    "v_exp_f32 v7, v103\n"
    "v_cvt_pk_bf16_f32 v148, v4, v5\n"
    "v_exp_f32 v2, v116\n"
    "v_exp_f32 v3, v117\n"
    "v_cvt_pk_bf16_f32 v149, v6, v7\n"
    "v_exp_f32 v4, v118\n"
    "v_exp_f32 v5, v119\n"
    "v_cvt_pk_bf16_f32 v150, v2, v3\n"
    "v_exp_f32 v6, v104\n"
    "v_exp_f32 v7, v105\n"
    "v_cvt_pk_bf16_f32 v151, v4, v5\n"
    "v_exp_f32 v2, v106\n"
    "v_exp_f32 v3, v107\n"
    "v_cvt_pk_bf16_f32 v152, v6, v7\n"
    "v_exp_f32 v4, v120\n"
    "v_exp_f32 v5, v121\n"
    "v_cvt_pk_bf16_f32 v153, v2, v3\n"
    "v_exp_f32 v6, v122\n"
    "v_exp_f32 v7, v123\n"
    "v_cvt_pk_bf16_f32 v154, v4, v5\n"
    "v_exp_f32 v2, v108\n"
    "v_exp_f32 v3, v109\n"
    "v_cvt_pk_bf16_f32 v155, v6, v7\n"
    "v_exp_f32 v4, v110\n"
    "v_exp_f32 v5, v111\n"
    "v_cvt_pk_bf16_f32 v156, v2, v3\n"
    "v_exp_f32 v6, v124\n"
    "v_exp_f32 v7, v125\n"
    "v_cvt_pk_bf16_f32 v157, v4, v5\n"
    "v_exp_f32 v2, v126\n"
    "v_exp_f32 v3, v127\n"
    "v_cvt_pk_bf16_f32 v158, v6, v7\n"
    "v_cvt_pk_bf16_f32 v159, v2, v3\n"
    "v_lshrrev_b32 v9, 4, v25\n"
    "v_mad_u32_u24 v9, v9, 48, s64\n"
    "s_nop 15\n"
    "s_mov_b64 vcc, -1\n"
    "s_setpc_b64 s[76:77]\n"
    ".Lf_RARET0:\n"
    "s_nop 15\n"
    "s_mov_b32 s73, 1\n"
    "v_mov_b32 v11, 126992\n"
    "ds_write_b32 v11, v11\n"
    "s_waitcnt lgkmcnt(0)\n"
    "v_lshrrev_b32 v11, 4, v25\n"
    "v_lshlrev_b32 v11, 4, v11\n"
    "v_and_b32 v13, 15, v25\n"
    "v_lshl_add_u32 v13, v13, 2, s68\n"
    "ds_read_b32 v15, v13\n"
    "s_waitcnt lgkmcnt(0)\n"
    "v_add_f32 v96, v96, v15\n"
    "v_add_f32 v97, v97, v15\n"
    "v_add_f32 v98, v98, v15\n"
    "v_add_f32 v99, v99, v15\n"
    "v_add_f32 v112, v112, v15\n"
    "v_add_f32 v113, v113, v15\n"
    "v_add_f32 v114, v114, v15\n"
    "v_add_f32 v115, v115, v15\n"
    "v_max3_f32 v43, v64, v65, s91\n"
    "v_max3_f32 v43, v43, v66, v67\n"
    "v_max3_f32 v43, v43, v80, v81\n"
    "v_max3_f32 v43, v43, v82, v83\n"
    "v_max3_f32 v43, v43, v96, v97\n"
    "v_max3_f32 v43, v43, v98, v99\n"
    "v_max3_f32 v43, v43, v112, v113\n"
    "v_max3_f32 v43, v43, v114, v115\n"
    "v_mov_b32 v12, v43\n"
    "s_nop 1\n"
    "v_permlane32_swap_b32 v43, v12\n"
    "s_nop 1\n"
    "v_max_f32 v43, v43, v12\n"
    "v_mov_b32 v12, v43\n"
    "s_nop 1\n"
    "v_permlane16_swap_b32 v43, v12\n"
    "s_nop 1\n"
    "v_max_f32 v43, v43, v12\n"
    "v_add_f32 v43, 0x41f00000, v43\n"
    "v_ceil_f32 v43, v43\n"
    "v_and_b32 v13, 15, v25\n"
    "v_lshl_add_u32 v13, v13, 2, s68\n"
    "ds_read_b32 v15, v13\n"
    "s_waitcnt lgkmcnt(0)\n"
    "v_sub_f32 v15, v15, v43\n"
    "ds_write_b32 v13, v15\n"
    "v_sub_f32 v64, v64, v43\n"
    "v_sub_f32 v65, v65, v43\n"
    "v_sub_f32 v66, v66, v43\n"
    "v_sub_f32 v67, v67, v43\n"
    "v_sub_f32 v80, v80, v43\n"
    "v_sub_f32 v81, v81, v43\n"
    "v_sub_f32 v82, v82, v43\n"
    "v_sub_f32 v83, v83, v43\n"
    "v_sub_f32 v96, v96, v43\n"
    "v_sub_f32 v97, v97, v43\n"
    "v_sub_f32 v98, v98, v43\n"
    "v_sub_f32 v99, v99, v43\n"
    "v_sub_f32 v112, v112, v43\n"
    "v_sub_f32 v113, v113, v43\n"
    "v_sub_f32 v114, v114, v43\n"
    "v_sub_f32 v115, v115, v43\n"
    "v_max_f32 v43, 0, v43\n"
    "v_exp_f32 v12, -v43\n"
    "s_waitcnt lgkmcnt(0)\n"
    "ds_bpermute_b32 v13, v11, v12 offset:0\n"
    "ds_bpermute_b32 v15, v11, v12 offset:4\n"
    "ds_bpermute_b32 v16, v11, v12 offset:8\n"
    "ds_bpermute_b32 v17, v11, v12 offset:12\n"
    "s_waitcnt lgkmcnt(0)\n"
    "v_mul_f32 v10, v10, v12\n"
    "v_lshlrev_b32 v41, 16, v144\n"
    "v_and_b32 v42, 0xffff0000, v144\n"
    "v_mul_f32 v41, v41, v12\n"
    "v_mul_f32 v42, v42, v12\n"
    "v_cvt_pk_bf16_f32 v144, v41, v42\n"
    "v_lshlrev_b32 v41, 16, v145\n"
    "v_and_b32 v42, 0xffff0000, v145\n"
    "v_mul_f32 v41, v41, v12\n"
    "v_mul_f32 v42, v42, v12\n"
    "v_cvt_pk_bf16_f32 v145, v41, v42\n"
    "v_lshlrev_b32 v41, 16, v146\n"
    "v_and_b32 v42, 0xffff0000, v146\n"
    "v_mul_f32 v41, v41, v12\n"
    "v_mul_f32 v42, v42, v12\n"
    "v_cvt_pk_bf16_f32 v146, v41, v42\n"
    "v_lshlrev_b32 v41, 16, v147\n"
    "v_and_b32 v42, 0xffff0000, v147\n"
    "v_mul_f32 v41, v41, v12\n"
    "v_mul_f32 v42, v42, v12\n"
    "v_cvt_pk_bf16_f32 v147, v41, v42\n"
    "v_accvgpr_read_b32 v41, a0\n"
    "v_mul_f32 v41, v41, v13\n"
    "v_accvgpr_write_b32 a0, v41\n"
    "v_accvgpr_read_b32 v42, a1\n"
    "v_mul_f32 v42, v42, v15\n"
    "v_accvgpr_write_b32 a1, v42\n"
    "v_accvgpr_read_b32 v41, a2\n"
    "v_mul_f32 v41, v41, v16\n"
    "v_accvgpr_write_b32 a2, v41\n"
    "v_accvgpr_read_b32 v42, a3\n"
    "v_mul_f32 v42, v42, v17\n"
    "v_accvgpr_write_b32 a3, v42\n"
    "v_accvgpr_read_b32 v41, a16\n"
    "v_mul_f32 v41, v41, v13\n"
    "v_accvgpr_write_b32 a16, v41\n"
    "v_accvgpr_read_b32 v42, a17\n"
    "v_mul_f32 v42, v42, v15\n"
    "v_accvgpr_write_b32 a17, v42\n"
    "v_accvgpr_read_b32 v41, a18\n"
    "v_mul_f32 v41, v41, v16\n"
    "v_accvgpr_write_b32 a18, v41\n"
    "v_accvgpr_read_b32 v42, a19\n"
    "v_mul_f32 v42, v42, v17\n"
    "v_accvgpr_write_b32 a19, v42\n"
    "v_accvgpr_read_b32 v41, a32\n"
    "v_mul_f32 v41, v41, v13\n"
    "v_accvgpr_write_b32 a32, v41\n"
    "v_accvgpr_read_b32 v42, a33\n"
    "v_mul_f32 v42, v42, v15\n"
    "v_accvgpr_write_b32 a33, v42\n"
    "v_accvgpr_read_b32 v41, a34\n"
    "v_mul_f32 v41, v41, v16\n"
    "v_accvgpr_write_b32 a34, v41\n"
    "v_accvgpr_read_b32 v42, a35\n"
    "v_mul_f32 v42, v42, v17\n"
    "v_accvgpr_write_b32 a35, v42\n"
    "v_accvgpr_read_b32 v41, a48\n"
    "v_mul_f32 v41, v41, v13\n"
    "v_accvgpr_write_b32 a48, v41\n"
    "v_accvgpr_read_b32 v42, a49\n"
    "v_mul_f32 v42, v42, v15\n"
    "v_accvgpr_write_b32 a49, v42\n"
    "v_accvgpr_read_b32 v41, a50\n"
    "v_mul_f32 v41, v41, v16\n"
    "v_accvgpr_write_b32 a50, v41\n"
    "v_accvgpr_read_b32 v42, a51\n"
    "v_mul_f32 v42, v42, v17\n"
    "v_accvgpr_write_b32 a51, v42\n"
    "v_accvgpr_read_b32 v41, a64\n"
    "v_mul_f32 v41, v41, v13\n"
    "v_accvgpr_write_b32 a64, v41\n"
    "v_accvgpr_read_b32 v42, a65\n"
    "v_mul_f32 v42, v42, v15\n"
    "v_accvgpr_write_b32 a65, v42\n"
    "v_accvgpr_read_b32 v41, a66\n"
    "v_mul_f32 v41, v41, v16\n"
    "v_accvgpr_write_b32 a66, v41\n"
    "v_accvgpr_read_b32 v42, a67\n"
    "v_mul_f32 v42, v42, v17\n"
    "v_accvgpr_write_b32 a67, v42\n"
    "v_accvgpr_read_b32 v41, a80\n"
    "v_mul_f32 v41, v41, v13\n"
    "v_accvgpr_write_b32 a80, v41\n"
    "v_accvgpr_read_b32 v42, a81\n"
    "v_mul_f32 v42, v42, v15\n"
    "v_accvgpr_write_b32 a81, v42\n"
    "v_accvgpr_read_b32 v41, a82\n"
    "v_mul_f32 v41, v41, v16\n"
    "v_accvgpr_write_b32 a82, v41\n"
    "v_accvgpr_read_b32 v42, a83\n"
    "v_mul_f32 v42, v42, v17\n"
    "v_accvgpr_write_b32 a83, v42\n"
    "v_accvgpr_read_b32 v41, a96\n"
    "v_mul_f32 v41, v41, v13\n"
    "v_accvgpr_write_b32 a96, v41\n"
    "v_accvgpr_read_b32 v42, a97\n"
    "v_mul_f32 v42, v42, v15\n"
    "v_accvgpr_write_b32 a97, v42\n"
    "v_accvgpr_read_b32 v41, a98\n"
    "v_mul_f32 v41, v41, v16\n"
    "v_accvgpr_write_b32 a98, v41\n"
    "v_accvgpr_read_b32 v42, a99\n"
    "v_mul_f32 v42, v42, v17\n"
    "v_accvgpr_write_b32 a99, v42\n"
    "v_accvgpr_read_b32 v41, a112\n"
    "v_mul_f32 v41, v41, v13\n"
    "v_accvgpr_write_b32 a112, v41\n"
    "v_accvgpr_read_b32 v42, a113\n"
    "v_mul_f32 v42, v42, v15\n"
    "v_accvgpr_write_b32 a113, v42\n"
    "v_accvgpr_read_b32 v41, a114\n"
    "v_mul_f32 v41, v41, v16\n"
    "v_accvgpr_write_b32 a114, v41\n"
    "v_accvgpr_read_b32 v42, a115\n"
    "v_mul_f32 v42, v42, v17\n"
    "v_accvgpr_write_b32 a115, v42\n"
    "v_exp_f32 v11, v64\n"
    "v_exp_f32 v12, v65\n"
    "v_exp_f32 v13, v66\n"
    "v_exp_f32 v15, v67\n"
    "v_cvt_pk_bf16_f32 v128, v11, v12\n"
    "v_exp_f32 v16, v80\n"
    "v_exp_f32 v17, v81\n"
    "v_cvt_pk_bf16_f32 v129, v13, v15\n"
    "v_exp_f32 v41, v82\n"
    "v_exp_f32 v42, v83\n"
    "v_cvt_pk_bf16_f32 v130, v16, v17\n"
    "v_exp_f32 v11, v96\n"
    "v_exp_f32 v12, v97\n"
    "v_cvt_pk_bf16_f32 v131, v41, v42\n"
    "v_exp_f32 v13, v98\n"
    "v_exp_f32 v15, v99\n"
    "v_cvt_pk_bf16_f32 v48, v11, v12\n"
    "v_exp_f32 v16, v112\n"
    "v_exp_f32 v17, v113\n"
    "v_cvt_pk_bf16_f32 v49, v13, v15\n"
    "v_exp_f32 v41, v114\n"
    "v_exp_f32 v42, v115\n"
    "v_cvt_pk_bf16_f32 v50, v16, v17\n"
    "v_cvt_pk_bf16_f32 v51, v41, v42\n"
    "s_nop 15\n"
    "s_mov_b64 vcc, -1\n"
    "s_setpc_b64 s[76:77]\n"
    ".Lf_RARET1:\n"
    "s_nop 15\n"
    "s_mov_b32 s73, 1\n"
    "v_mov_b32 v11, 126992\n"
    "ds_write_b32 v11, v11\n"
    "s_waitcnt lgkmcnt(0)\n"
    "v_lshrrev_b32 v11, 4, v25\n"
    "v_lshlrev_b32 v11, 4, v11\n"
    "v_and_b32 v13, 15, v25\n"
    "v_lshl_add_u32 v13, v13, 2, s70\n"
    "ds_read_b32 v15, v13\n"
    "s_waitcnt lgkmcnt(0)\n"
    "v_add_f32 v100, v100, v15\n"
    "v_add_f32 v101, v101, v15\n"
    "v_add_f32 v102, v102, v15\n"
    "v_add_f32 v103, v103, v15\n"
    "v_add_f32 v116, v116, v15\n"
    "v_add_f32 v117, v117, v15\n"
    "v_add_f32 v118, v118, v15\n"
    "v_add_f32 v119, v119, v15\n"
    "v_max3_f32 v43, v68, v69, s91\n"
    "v_max3_f32 v43, v43, v70, v71\n"
    "v_max3_f32 v43, v43, v84, v85\n"
    "v_max3_f32 v43, v43, v86, v87\n"
    "v_max3_f32 v43, v43, v100, v101\n"
    "v_max3_f32 v43, v43, v102, v103\n"
    "v_max3_f32 v43, v43, v116, v117\n"
    "v_max3_f32 v43, v43, v118, v119\n"
    "v_mov_b32 v12, v43\n"
    "s_nop 1\n"
    "v_permlane32_swap_b32 v43, v12\n"
    "s_nop 1\n"
    "v_max_f32 v43, v43, v12\n"
    "v_mov_b32 v12, v43\n"
    "s_nop 1\n"
    "v_permlane16_swap_b32 v43, v12\n"
    "s_nop 1\n"
    "v_max_f32 v43, v43, v12\n"
    "v_add_f32 v43, 0x41f00000, v43\n"
    "v_ceil_f32 v43, v43\n"
    "v_and_b32 v13, 15, v25\n"
    "v_lshl_add_u32 v13, v13, 2, s70\n"
    "ds_read_b32 v15, v13\n"
    "s_waitcnt lgkmcnt(0)\n"
    "v_sub_f32 v15, v15, v43\n"
    "ds_write_b32 v13, v15\n"
    "v_sub_f32 v68, v68, v43\n"
    "v_sub_f32 v69, v69, v43\n"
    "v_sub_f32 v70, v70, v43\n"
    "v_sub_f32 v71, v71, v43\n"
    "v_sub_f32 v84, v84, v43\n"
    "v_sub_f32 v85, v85, v43\n"
    "v_sub_f32 v86, v86, v43\n"
    "v_sub_f32 v87, v87, v43\n"
    "v_sub_f32 v100, v100, v43\n"
    "v_sub_f32 v101, v101, v43\n"
    "v_sub_f32 v102, v102, v43\n"
    "v_sub_f32 v103, v103, v43\n"
    "v_sub_f32 v116, v116, v43\n"
    "v_sub_f32 v117, v117, v43\n"
    "v_sub_f32 v118, v118, v43\n"
    "v_sub_f32 v119, v119, v43\n"
    "v_max_f32 v43, 0, v43\n"
    "v_exp_f32 v12, -v43\n"
    "s_waitcnt lgkmcnt(0)\n"
    "ds_bpermute_b32 v13, v11, v12 offset:0\n"
    "ds_bpermute_b32 v15, v11, v12 offset:4\n"
    "ds_bpermute_b32 v16, v11, v12 offset:8\n"
    "ds_bpermute_b32 v17, v11, v12 offset:12\n"
    "s_waitcnt lgkmcnt(0)\n"
    "v_mul_f32 v14, v14, v12\n"
    "v_lshlrev_b32 v41, 16, v148\n"
    "v_and_b32 v42, 0xffff0000, v148\n"
    "v_mul_f32 v41, v41, v12\n"
    "v_mul_f32 v42, v42, v12\n"
    "v_cvt_pk_bf16_f32 v148, v41, v42\n"
    "v_lshlrev_b32 v41, 16, v149\n"
    "v_and_b32 v42, 0xffff0000, v149\n"
    "v_mul_f32 v41, v41, v12\n"
    "v_mul_f32 v42, v42, v12\n"
    "v_cvt_pk_bf16_f32 v149, v41, v42\n"
    "v_lshlrev_b32 v41, 16, v150\n"
    "v_and_b32 v42, 0xffff0000, v150\n"
    "v_mul_f32 v41, v41, v12\n"
    "v_mul_f32 v42, v42, v12\n"
    "v_cvt_pk_bf16_f32 v150, v41, v42\n"
    "v_lshlrev_b32 v41, 16, v151\n"
    "v_and_b32 v42, 0xffff0000, v151\n"
    "v_mul_f32 v41, v41, v12\n"
    "v_mul_f32 v42, v42, v12\n"
    "v_cvt_pk_bf16_f32 v151, v41, v42\n"
    "v_accvgpr_read_b32 v41, a4\n"
    "v_mul_f32 v41, v41, v13\n"
    "v_accvgpr_write_b32 a4, v41\n"
    "v_accvgpr_read_b32 v42, a5\n"
    "v_mul_f32 v42, v42, v15\n"
    "v_accvgpr_write_b32 a5, v42\n"
    "v_accvgpr_read_b32 v41, a6\n"
    "v_mul_f32 v41, v41, v16\n"
    "v_accvgpr_write_b32 a6, v41\n"
    "v_accvgpr_read_b32 v42, a7\n"
    "v_mul_f32 v42, v42, v17\n"
    "v_accvgpr_write_b32 a7, v42\n"
    "v_accvgpr_read_b32 v41, a20\n"
    "v_mul_f32 v41, v41, v13\n"
    "v_accvgpr_write_b32 a20, v41\n"
    "v_accvgpr_read_b32 v42, a21\n"
    "v_mul_f32 v42, v42, v15\n"
    "v_accvgpr_write_b32 a21, v42\n"
    "v_accvgpr_read_b32 v41, a22\n"
    "v_mul_f32 v41, v41, v16\n"
    "v_accvgpr_write_b32 a22, v41\n"
    "v_accvgpr_read_b32 v42, a23\n"
    "v_mul_f32 v42, v42, v17\n"
    "v_accvgpr_write_b32 a23, v42\n"
    "v_accvgpr_read_b32 v41, a36\n"
    "v_mul_f32 v41, v41, v13\n"
    "v_accvgpr_write_b32 a36, v41\n"
    "v_accvgpr_read_b32 v42, a37\n"
    "v_mul_f32 v42, v42, v15\n"
    "v_accvgpr_write_b32 a37, v42\n"
    "v_accvgpr_read_b32 v41, a38\n"
    "v_mul_f32 v41, v41, v16\n"
    "v_accvgpr_write_b32 a38, v41\n"
    "v_accvgpr_read_b32 v42, a39\n"
    "v_mul_f32 v42, v42, v17\n"
    "v_accvgpr_write_b32 a39, v42\n"
    "v_accvgpr_read_b32 v41, a52\n"
    "v_mul_f32 v41, v41, v13\n"
    "v_accvgpr_write_b32 a52, v41\n"
    "v_accvgpr_read_b32 v42, a53\n"
    "v_mul_f32 v42, v42, v15\n"
    "v_accvgpr_write_b32 a53, v42\n"
    "v_accvgpr_read_b32 v41, a54\n"
    "v_mul_f32 v41, v41, v16\n"
    "v_accvgpr_write_b32 a54, v41\n"
    "v_accvgpr_read_b32 v42, a55\n"
    "v_mul_f32 v42, v42, v17\n"
    "v_accvgpr_write_b32 a55, v42\n"
    "v_accvgpr_read_b32 v41, a68\n"
    "v_mul_f32 v41, v41, v13\n"
    "v_accvgpr_write_b32 a68, v41\n"
    "v_accvgpr_read_b32 v42, a69\n"
    "v_mul_f32 v42, v42, v15\n"
    "v_accvgpr_write_b32 a69, v42\n"
    "v_accvgpr_read_b32 v41, a70\n"
    "v_mul_f32 v41, v41, v16\n"
    "v_accvgpr_write_b32 a70, v41\n"
    "v_accvgpr_read_b32 v42, a71\n"
    "v_mul_f32 v42, v42, v17\n"
    "v_accvgpr_write_b32 a71, v42\n"
    "v_accvgpr_read_b32 v41, a84\n"
    "v_mul_f32 v41, v41, v13\n"
    "v_accvgpr_write_b32 a84, v41\n"
    "v_accvgpr_read_b32 v42, a85\n"
    "v_mul_f32 v42, v42, v15\n"
    "v_accvgpr_write_b32 a85, v42\n"
    "v_accvgpr_read_b32 v41, a86\n"
    "v_mul_f32 v41, v41, v16\n"
    "v_accvgpr_write_b32 a86, v41\n"
    "v_accvgpr_read_b32 v42, a87\n"
    "v_mul_f32 v42, v42, v17\n"
    "v_accvgpr_write_b32 a87, v42\n"
    "v_accvgpr_read_b32 v41, a100\n"
    "v_mul_f32 v41, v41, v13\n"
    "v_accvgpr_write_b32 a100, v41\n"
    "v_accvgpr_read_b32 v42, a101\n"
    "v_mul_f32 v42, v42, v15\n"
    "v_accvgpr_write_b32 a101, v42\n"
    "v_accvgpr_read_b32 v41, a102\n"
    "v_mul_f32 v41, v41, v16\n"
    "v_accvgpr_write_b32 a102, v41\n"
    "v_accvgpr_read_b32 v42, a103\n"
    "v_mul_f32 v42, v42, v17\n"
    "v_accvgpr_write_b32 a103, v42\n"
    "v_accvgpr_read_b32 v41, a116\n"
    "v_mul_f32 v41, v41, v13\n"
    "v_accvgpr_write_b32 a116, v41\n"
    "v_accvgpr_read_b32 v42, a117\n"
    "v_mul_f32 v42, v42, v15\n"
    "v_accvgpr_write_b32 a117, v42\n"
    "v_accvgpr_read_b32 v41, a118\n"
    "v_mul_f32 v41, v41, v16\n"
    "v_accvgpr_write_b32 a118, v41\n"
    "v_accvgpr_read_b32 v42, a119\n"
    "v_mul_f32 v42, v42, v17\n"
    "v_accvgpr_write_b32 a119, v42\n"
    "v_exp_f32 v11, v68\n"
    "v_exp_f32 v12, v69\n"
    "v_exp_f32 v13, v70\n"
    "v_exp_f32 v15, v71\n"
    "v_cvt_pk_bf16_f32 v132, v11, v12\n"
    "v_exp_f32 v16, v84\n"
    "v_exp_f32 v17, v85\n"
    "v_cvt_pk_bf16_f32 v133, v13, v15\n"
    "v_exp_f32 v41, v86\n"
    "v_exp_f32 v42, v87\n"
    "v_cvt_pk_bf16_f32 v134, v16, v17\n"
    "v_exp_f32 v11, v100\n"
    "v_exp_f32 v12, v101\n"
    "v_cvt_pk_bf16_f32 v135, v41, v42\n"
    "v_exp_f32 v13, v102\n"
    "v_exp_f32 v15, v103\n"
    "v_cvt_pk_bf16_f32 v52, v11, v12\n"
    "v_exp_f32 v16, v116\n"
    "v_exp_f32 v17, v117\n"
    "v_cvt_pk_bf16_f32 v53, v13, v15\n"
    "v_exp_f32 v41, v118\n"
    "v_exp_f32 v42, v119\n"
    "v_cvt_pk_bf16_f32 v54, v16, v17\n"
    "v_cvt_pk_bf16_f32 v55, v41, v42\n"
    "s_nop 15\n"
    "s_mov_b64 vcc, -1\n"
    "s_setpc_b64 s[76:77]\n"
    ".Lf_RARET2:\n"
    "s_nop 15\n"
    "s_mov_b32 s73, 1\n"
    "v_mov_b32 v11, 126992\n"
    "ds_write_b32 v11, v11\n"
    "s_waitcnt lgkmcnt(0)\n"
    "v_lshrrev_b32 v11, 4, v25\n"
    "v_lshlrev_b32 v11, 4, v11\n"
    "v_and_b32 v13, 15, v25\n"
    "v_lshl_add_u32 v13, v13, 2, s71\n"
    "ds_read_b32 v15, v13\n"
    "s_waitcnt lgkmcnt(0)\n"
    "v_add_f32 v104, v104, v15\n"
    "v_add_f32 v105, v105, v15\n"
    "v_add_f32 v106, v106, v15\n"
    "v_add_f32 v107, v107, v15\n"
    "v_add_f32 v120, v120, v15\n"
    "v_add_f32 v121, v121, v15\n"
    "v_add_f32 v122, v122, v15\n"
    "v_add_f32 v123, v123, v15\n"
    "v_max3_f32 v43, v72, v73, s91\n"
    "v_max3_f32 v43, v43, v74, v75\n"
    "v_max3_f32 v43, v43, v88, v89\n"
    "v_max3_f32 v43, v43, v90, v91\n"
    "v_max3_f32 v43, v43, v104, v105\n"
    "v_max3_f32 v43, v43, v106, v107\n"
    "v_max3_f32 v43, v43, v120, v121\n"
    "v_max3_f32 v43, v43, v122, v123\n"
    "v_mov_b32 v12, v43\n"
    "s_nop 1\n"
    "v_permlane32_swap_b32 v43, v12\n"
    "s_nop 1\n"
    "v_max_f32 v43, v43, v12\n"
    "v_mov_b32 v12, v43\n"
    "s_nop 1\n"
    "v_permlane16_swap_b32 v43, v12\n"
    "s_nop 1\n"
    "v_max_f32 v43, v43, v12\n"
    "v_add_f32 v43, 0x41f00000, v43\n"
    "v_ceil_f32 v43, v43\n"
    "v_and_b32 v13, 15, v25\n"
    "v_lshl_add_u32 v13, v13, 2, s71\n"
    "ds_read_b32 v15, v13\n"
    "s_waitcnt lgkmcnt(0)\n"
    "v_sub_f32 v15, v15, v43\n"
    "ds_write_b32 v13, v15\n"
    "v_sub_f32 v72, v72, v43\n"
    "v_sub_f32 v73, v73, v43\n"
    "v_sub_f32 v74, v74, v43\n"
    "v_sub_f32 v75, v75, v43\n"
    "v_sub_f32 v88, v88, v43\n"
    "v_sub_f32 v89, v89, v43\n"
    "v_sub_f32 v90, v90, v43\n"
    "v_sub_f32 v91, v91, v43\n"
    "v_sub_f32 v104, v104, v43\n"
    "v_sub_f32 v105, v105, v43\n"
    "v_sub_f32 v106, v106, v43\n"
    "v_sub_f32 v107, v107, v43\n"
    "v_sub_f32 v120, v120, v43\n"
    "v_sub_f32 v121, v121, v43\n"
    "v_sub_f32 v122, v122, v43\n"
    "v_sub_f32 v123, v123, v43\n"
    "v_max_f32 v43, 0, v43\n"
    "v_exp_f32 v12, -v43\n"
    "s_waitcnt lgkmcnt(0)\n"
    "ds_bpermute_b32 v13, v11, v12 offset:0\n"
    "ds_bpermute_b32 v15, v11, v12 offset:4\n"
    "ds_bpermute_b32 v16, v11, v12 offset:8\n"
    "ds_bpermute_b32 v17, v11, v12 offset:12\n"
    "s_waitcnt lgkmcnt(0)\n"
    "v_mul_f32 v40, v40, v12\n"
    "v_lshlrev_b32 v41, 16, v152\n"
    "v_and_b32 v42, 0xffff0000, v152\n"
    "v_mul_f32 v41, v41, v12\n"
    "v_mul_f32 v42, v42, v12\n"
    "v_cvt_pk_bf16_f32 v152, v41, v42\n"
    "v_lshlrev_b32 v41, 16, v153\n"
    "v_and_b32 v42, 0xffff0000, v153\n"
    "v_mul_f32 v41, v41, v12\n"
    "v_mul_f32 v42, v42, v12\n"
    "v_cvt_pk_bf16_f32 v153, v41, v42\n"
    "v_lshlrev_b32 v41, 16, v154\n"
    "v_and_b32 v42, 0xffff0000, v154\n"
    "v_mul_f32 v41, v41, v12\n"
    "v_mul_f32 v42, v42, v12\n"
    "v_cvt_pk_bf16_f32 v154, v41, v42\n"
    "v_lshlrev_b32 v41, 16, v155\n"
    "v_and_b32 v42, 0xffff0000, v155\n"
    "v_mul_f32 v41, v41, v12\n"
    "v_mul_f32 v42, v42, v12\n"
    "v_cvt_pk_bf16_f32 v155, v41, v42\n"
    "v_accvgpr_read_b32 v41, a8\n"
    "v_mul_f32 v41, v41, v13\n"
    "v_accvgpr_write_b32 a8, v41\n"
    "v_accvgpr_read_b32 v42, a9\n"
    "v_mul_f32 v42, v42, v15\n"
    "v_accvgpr_write_b32 a9, v42\n"
    "v_accvgpr_read_b32 v41, a10\n"
    "v_mul_f32 v41, v41, v16\n"
    "v_accvgpr_write_b32 a10, v41\n"
    "v_accvgpr_read_b32 v42, a11\n"
    "v_mul_f32 v42, v42, v17\n"
    "v_accvgpr_write_b32 a11, v42\n"
    "v_accvgpr_read_b32 v41, a24\n"
    "v_mul_f32 v41, v41, v13\n"
    "v_accvgpr_write_b32 a24, v41\n"
    "v_accvgpr_read_b32 v42, a25\n"
    "v_mul_f32 v42, v42, v15\n"
    "v_accvgpr_write_b32 a25, v42\n"
    "v_accvgpr_read_b32 v41, a26\n"
    "v_mul_f32 v41, v41, v16\n"
    "v_accvgpr_write_b32 a26, v41\n"
    "v_accvgpr_read_b32 v42, a27\n"
    "v_mul_f32 v42, v42, v17\n"
    "v_accvgpr_write_b32 a27, v42\n"
    "v_accvgpr_read_b32 v41, a40\n"
    "v_mul_f32 v41, v41, v13\n"
    "v_accvgpr_write_b32 a40, v41\n"
    "v_accvgpr_read_b32 v42, a41\n"
    "v_mul_f32 v42, v42, v15\n"
    "v_accvgpr_write_b32 a41, v42\n"
    "v_accvgpr_read_b32 v41, a42\n"
    "v_mul_f32 v41, v41, v16\n"
    "v_accvgpr_write_b32 a42, v41\n"
    "v_accvgpr_read_b32 v42, a43\n"
    "v_mul_f32 v42, v42, v17\n"
    "v_accvgpr_write_b32 a43, v42\n"
    "v_accvgpr_read_b32 v41, a56\n"
    "v_mul_f32 v41, v41, v13\n"
    "v_accvgpr_write_b32 a56, v41\n"
    "v_accvgpr_read_b32 v42, a57\n"
    "v_mul_f32 v42, v42, v15\n"
    "v_accvgpr_write_b32 a57, v42\n"
    "v_accvgpr_read_b32 v41, a58\n"
    "v_mul_f32 v41, v41, v16\n"
    "v_accvgpr_write_b32 a58, v41\n"
    "v_accvgpr_read_b32 v42, a59\n"
    "v_mul_f32 v42, v42, v17\n"
    "v_accvgpr_write_b32 a59, v42\n"
    "v_accvgpr_read_b32 v41, a72\n"
    "v_mul_f32 v41, v41, v13\n"
    "v_accvgpr_write_b32 a72, v41\n"
    "v_accvgpr_read_b32 v42, a73\n"
    "v_mul_f32 v42, v42, v15\n"
    "v_accvgpr_write_b32 a73, v42\n"
    "v_accvgpr_read_b32 v41, a74\n"
    "v_mul_f32 v41, v41, v16\n"
    "v_accvgpr_write_b32 a74, v41\n"
    "v_accvgpr_read_b32 v42, a75\n"
    "v_mul_f32 v42, v42, v17\n"
    "v_accvgpr_write_b32 a75, v42\n"
    "v_accvgpr_read_b32 v41, a88\n"
    "v_mul_f32 v41, v41, v13\n"
    "v_accvgpr_write_b32 a88, v41\n"
    "v_accvgpr_read_b32 v42, a89\n"
    "v_mul_f32 v42, v42, v15\n"
    "v_accvgpr_write_b32 a89, v42\n"
    "v_accvgpr_read_b32 v41, a90\n"
    "v_mul_f32 v41, v41, v16\n"
    "v_accvgpr_write_b32 a90, v41\n"
    "v_accvgpr_read_b32 v42, a91\n"
    "v_mul_f32 v42, v42, v17\n"
    "v_accvgpr_write_b32 a91, v42\n"
    "v_accvgpr_read_b32 v41, a104\n"
    "v_mul_f32 v41, v41, v13\n"
    "v_accvgpr_write_b32 a104, v41\n"
    "v_accvgpr_read_b32 v42, a105\n"
    "v_mul_f32 v42, v42, v15\n"
    "v_accvgpr_write_b32 a105, v42\n"
    "v_accvgpr_read_b32 v41, a106\n"
    "v_mul_f32 v41, v41, v16\n"
    "v_accvgpr_write_b32 a106, v41\n"
    "v_accvgpr_read_b32 v42, a107\n"
    "v_mul_f32 v42, v42, v17\n"
    "v_accvgpr_write_b32 a107, v42\n"
    "v_accvgpr_read_b32 v41, a120\n"
    "v_mul_f32 v41, v41, v13\n"
    "v_accvgpr_write_b32 a120, v41\n"
    "v_accvgpr_read_b32 v42, a121\n"
    "v_mul_f32 v42, v42, v15\n"
    "v_accvgpr_write_b32 a121, v42\n"
    "v_accvgpr_read_b32 v41, a122\n"
    "v_mul_f32 v41, v41, v16\n"
    "v_accvgpr_write_b32 a122, v41\n"
    "v_accvgpr_read_b32 v42, a123\n"
    "v_mul_f32 v42, v42, v17\n"
    "v_accvgpr_write_b32 a123, v42\n"
    "v_exp_f32 v11, v72\n"
    "v_exp_f32 v12, v73\n"
    "v_exp_f32 v13, v74\n"
    "v_exp_f32 v15, v75\n"
    "v_cvt_pk_bf16_f32 v136, v11, v12\n"
    "v_exp_f32 v16, v88\n"
    "v_exp_f32 v17, v89\n"
    "v_cvt_pk_bf16_f32 v137, v13, v15\n"
    "v_exp_f32 v41, v90\n"
    "v_exp_f32 v42, v91\n"
    "v_cvt_pk_bf16_f32 v138, v16, v17\n"
    "v_exp_f32 v11, v104\n"
    "v_exp_f32 v12, v105\n"
    "v_cvt_pk_bf16_f32 v139, v41, v42\n"
    "v_exp_f32 v13, v106\n"
    "v_exp_f32 v15, v107\n"
    "v_cvt_pk_bf16_f32 v56, v11, v12\n"
    "v_exp_f32 v16, v120\n"
    "v_exp_f32 v17, v121\n"
    "v_cvt_pk_bf16_f32 v57, v13, v15\n"
    "v_exp_f32 v41, v122\n"
    "v_exp_f32 v42, v123\n"
    "v_cvt_pk_bf16_f32 v58, v16, v17\n"
    "v_cvt_pk_bf16_f32 v59, v41, v42\n"
    "s_nop 15\n"
    "s_mov_b64 vcc, -1\n"
    "s_setpc_b64 s[76:77]\n"
    ".Lf_RARET3:\n"
    "s_nop 15\n"
    "s_mov_b32 s73, 1\n"
    "v_mov_b32 v11, 126992\n"
    "ds_write_b32 v11, v11\n"
    "s_waitcnt lgkmcnt(0)\n"
    "v_lshrrev_b32 v11, 4, v25\n"
    "v_lshlrev_b32 v11, 4, v11\n"
    "v_and_b32 v13, 15, v25\n"
    "v_lshl_add_u32 v13, v13, 2, s86\n"
    "ds_read_b32 v15, v13\n"
    "s_waitcnt lgkmcnt(0)\n"
    "v_add_f32 v108, v108, v15\n"
    "v_add_f32 v109, v109, v15\n"
    "v_add_f32 v110, v110, v15\n"
    "v_add_f32 v111, v111, v15\n"
    "v_add_f32 v124, v124, v15\n"
    "v_add_f32 v125, v125, v15\n"
    "v_add_f32 v126, v126, v15\n"
    "v_add_f32 v127, v127, v15\n"
    "v_max3_f32 v43, v76, v77, s91\n"
    "v_max3_f32 v43, v43, v78, v79\n"
    "v_max3_f32 v43, v43, v92, v93\n"
    "v_max3_f32 v43, v43, v94, v95\n"
    "v_max3_f32 v43, v43, v108, v109\n"
    "v_max3_f32 v43, v43, v110, v111\n"
    "v_max3_f32 v43, v43, v124, v125\n"
    "v_max3_f32 v43, v43, v126, v127\n"
    "v_mov_b32 v12, v43\n"
    "s_nop 1\n"
    "v_permlane32_swap_b32 v43, v12\n"
    "s_nop 1\n"
    "v_max_f32 v43, v43, v12\n"
    "v_mov_b32 v12, v43\n"
    "s_nop 1\n"
    "v_permlane16_swap_b32 v43, v12\n"
    "s_nop 1\n"
    "v_max_f32 v43, v43, v12\n"
    "v_add_f32 v43, 0x41f00000, v43\n"
    "v_ceil_f32 v43, v43\n"
    "v_and_b32 v13, 15, v25\n"
    "v_lshl_add_u32 v13, v13, 2, s86\n"
    "ds_read_b32 v15, v13\n"
    "s_waitcnt lgkmcnt(0)\n"
    "v_sub_f32 v15, v15, v43\n"
    "ds_write_b32 v13, v15\n"
    "v_sub_f32 v76, v76, v43\n"
    "v_sub_f32 v77, v77, v43\n"
    "v_sub_f32 v78, v78, v43\n"
    "v_sub_f32 v79, v79, v43\n"
    "v_sub_f32 v92, v92, v43\n"
    "v_sub_f32 v93, v93, v43\n"
    "v_sub_f32 v94, v94, v43\n"
    "v_sub_f32 v95, v95, v43\n"
    "v_sub_f32 v108, v108, v43\n"
    "v_sub_f32 v109, v109, v43\n"
    "v_sub_f32 v110, v110, v43\n"
    "v_sub_f32 v111, v111, v43\n"
    "v_sub_f32 v124, v124, v43\n"
    "v_sub_f32 v125, v125, v43\n"
    "v_sub_f32 v126, v126, v43\n"
    "v_sub_f32 v127, v127, v43\n"
    "v_max_f32 v43, 0, v43\n"
    "v_exp_f32 v12, -v43\n"
    "s_waitcnt lgkmcnt(0)\n"
    "ds_bpermute_b32 v13, v11, v12 offset:0\n"
    "ds_bpermute_b32 v15, v11, v12 offset:4\n"
    "ds_bpermute_b32 v16, v11, v12 offset:8\n"
    "ds_bpermute_b32 v17, v11, v12 offset:12\n"
    "s_waitcnt lgkmcnt(0)\n"
    "v_mul_f32 v44, v44, v12\n"
    "v_lshlrev_b32 v41, 16, v156\n"
    "v_and_b32 v42, 0xffff0000, v156\n"
    "v_mul_f32 v41, v41, v12\n"
    "v_mul_f32 v42, v42, v12\n"
    "v_cvt_pk_bf16_f32 v156, v41, v42\n"
    "v_lshlrev_b32 v41, 16, v157\n"
    "v_and_b32 v42, 0xffff0000, v157\n"
    "v_mul_f32 v41, v41, v12\n"
    "v_mul_f32 v42, v42, v12\n"
    "v_cvt_pk_bf16_f32 v157, v41, v42\n"
    "v_lshlrev_b32 v41, 16, v158\n"
    "v_and_b32 v42, 0xffff0000, v158\n"
    "v_mul_f32 v41, v41, v12\n"
    "v_mul_f32 v42, v42, v12\n"
    "v_cvt_pk_bf16_f32 v158, v41, v42\n"
    "v_lshlrev_b32 v41, 16, v159\n"
    "v_and_b32 v42, 0xffff0000, v159\n"
    "v_mul_f32 v41, v41, v12\n"
    "v_mul_f32 v42, v42, v12\n"
    "v_cvt_pk_bf16_f32 v159, v41, v42\n"
    "v_mul_f32 v160, v160, v13\n"
    "v_mul_f32 v161, v161, v15\n"
    "v_mul_f32 v162, v162, v16\n"
    "v_mul_f32 v163, v163, v17\n"
    "v_mul_f32 v164, v164, v13\n"
    "v_mul_f32 v165, v165, v15\n"
    "v_mul_f32 v166, v166, v16\n"
    "v_mul_f32 v167, v167, v17\n"
    "v_mul_f32 v168, v168, v13\n"
    "v_mul_f32 v169, v169, v15\n"
    "v_mul_f32 v170, v170, v16\n"
    "v_mul_f32 v171, v171, v17\n"
    "v_mul_f32 v172, v172, v13\n"
    "v_mul_f32 v173, v173, v15\n"
    "v_mul_f32 v174, v174, v16\n"
    "v_mul_f32 v175, v175, v17\n"
    "v_mul_f32 v176, v176, v13\n"
    "v_mul_f32 v177, v177, v15\n"
    "v_mul_f32 v178, v178, v16\n"
    "v_mul_f32 v179, v179, v17\n"
    "v_mul_f32 v180, v180, v13\n"
    "v_mul_f32 v181, v181, v15\n"
    "v_mul_f32 v182, v182, v16\n"
    "v_mul_f32 v183, v183, v17\n"
    "v_mul_f32 v184, v184, v13\n"
    "v_mul_f32 v185, v185, v15\n"
    "v_mul_f32 v186, v186, v16\n"
    "v_mul_f32 v187, v187, v17\n"
    "v_mul_f32 v188, v188, v13\n"
    "v_mul_f32 v189, v189, v15\n"
    "v_mul_f32 v190, v190, v16\n"
    "v_mul_f32 v191, v191, v17\n"
    "v_exp_f32 v11, v76\n"
    "v_exp_f32 v12, v77\n"
    "v_exp_f32 v13, v78\n"
    "v_exp_f32 v15, v79\n"
    "v_cvt_pk_bf16_f32 v140, v11, v12\n"
    "v_exp_f32 v16, v92\n"
    "v_exp_f32 v17, v93\n"
    "v_cvt_pk_bf16_f32 v141, v13, v15\n"
    "v_exp_f32 v41, v94\n"
    "v_exp_f32 v42, v95\n"
    "v_cvt_pk_bf16_f32 v142, v16, v17\n"
    "v_exp_f32 v11, v108\n"
    "v_exp_f32 v12, v109\n"
    "v_cvt_pk_bf16_f32 v143, v41, v42\n"
    "v_exp_f32 v13, v110\n"
    "v_exp_f32 v15, v111\n"
    "v_cvt_pk_bf16_f32 v60, v11, v12\n"
    "v_exp_f32 v16, v124\n"
    "v_exp_f32 v17, v125\n"
    "v_cvt_pk_bf16_f32 v61, v13, v15\n"
    "v_exp_f32 v41, v126\n"
    "v_exp_f32 v42, v127\n"
    "v_cvt_pk_bf16_f32 v62, v16, v17\n"
    "v_cvt_pk_bf16_f32 v63, v41, v42\n"
    "s_nop 15\n"
    "s_mov_b64 vcc, -1\n"
    "s_setpc_b64 s[76:77]\n"
    :
    : [karg] "s"(karg), [bid] "s"(blockIdx.x), [w] "s"(w)
    : "memory", "scc", "vcc", "exec", "v0", "v1", "v2", "v3", "v4", "v5", "v6", "v7", "v8", "v9", "v10", "v11", "v12", "v13", "v14", "v15", "v16", "v17", "v18", "v19", "v20", "v21", "v22", "v23", "v24", "v25", "v26", "v27", "v28", "v29", "v30", "v31", "v32", "v33", "v34", "v35", "v36", "v37", "v38", "v39", "v40", "v41", "v42", "v43", "v44", "v45", "v46", "v47", "v48", "v49", "v50", "v51", "v52", "v53", "v54", "v55", "v56", "v57", "v58", "v59", "v60", "v61", "v62", "v63", "v64", "v65", "v66", "v67", "v68", "v69", "v70", "v71", "v72", "v73", "v74", "v75", "v76", "v77", "v78", "v79", "v80", "v81", "v82", "v83", "v84", "v85", "v86", "v87", "v88", "v89", "v90", "v91", "v92", "v93", "v94", "v95", "v96", "v97", "v98", "v99", "v100", "v101", "v102", "v103", "v104", "v105", "v106", "v107", "v108", "v109", "v110", "v111", "v112", "v113", "v114", "v115", "v116", "v117", "v118", "v119", "v120", "v121", "v122", "v123", "v124", "v125", "v126", "v127", "v128", "v129", "v130", "v131", "v132", "v133", "v134", "v135", "v136", "v137", "v138", "v139", "v140", "v141", "v142", "v143", "v144", "v145", "v146", "v147", "v148", "v149", "v150", "v151", "v152", "v153", "v154", "v155", "v156", "v157", "v158", "v159", "v160", "v161", "v162", "v163", "v164", "v165", "v166", "v167", "v168", "v169", "v170", "v171", "v172", "v173", "v174", "v175", "v176", "v177", "v178", "v179", "v180", "v181", "v182", "v183", "v184", "v185", "v186", "v187", "v188", "v189", "v190", "v191", "v192", "v193", "v194", "v195", "v196", "v197", "v198", "v199", "v200", "v201", "v202", "v203", "v204", "v205", "v206", "v207", "v208", "v209", "v210", "v211", "v212", "v213", "v214", "v215", "v216", "v217", "v218", "v219", "v220", "v221", "v222", "v223", "v224", "v225", "v226", "v227", "v228", "v229", "v230", "v231", "v232", "v233", "v234", "v235", "v236", "v237", "v238", "v239", "v240", "v241", "v242", "v243", "v244", "v245", "v246", "v247", "v248", "v249", "v250", "v251", "v252", "v253", "v254", "v255", "a0", "a1", "a2", "a3", "a4", "a5", "a6", "a7", "a8", "a9", "a10", "a11", "a12", "a13", "a14", "a15", "a16", "a17", "a18", "a19", "a20", "a21", "a22", "a23", "a24", "a25", "a26", "a27", "a28", "a29", "a30", "a31", "a32", "a33", "a34", "a35", "a36", "a37", "a38", "a39", "a40", "a41", "a42", "a43", "a44", "a45", "a46", "a47", "a48", "a49", "a50", "a51", "a52", "a53", "a54", "a55", "a56", "a57", "a58", "a59", "a60", "a61", "a62", "a63", "a64", "a65", "a66", "a67", "a68", "a69", "a70", "a71", "a72", "a73", "a74", "a75", "a76", "a77", "a78", "a79", "a80", "a81", "a82", "a83", "a84", "a85", "a86", "a87", "a88", "a89", "a90", "a91", "a92", "a93", "a94", "a95", "a96", "a97", "a98", "a99", "a100", "a101", "a102", "a103", "a104", "a105", "a106", "a107", "a108", "a109", "a110", "a111", "a112", "a113", "a114", "a115", "a116", "a117", "a118", "a119", "a120", "a121", "a122", "a123", "a124", "a125", "a126", "a127", "a128", "a129", "a130", "a131", "a132", "a133", "a134", "a135", "a136", "a137", "a138", "a139", "a140", "a141", "a142", "a143", "a144", "a145", "a146", "a147", "a148", "a149", "a150", "a151", "a152", "a153", "a154", "a155", "a156", "a157", "a158", "a159", "a160", "a161", "a162", "a163", "a164", "a165", "a166", "a167", "a168", "a169", "a170", "a171", "a172", "a173", "a174", "a175", "a176", "a177", "a178", "a179", "a180", "a181", "a182", "a183", "a184", "a185", "a186", "a187", "a188", "a189", "a190", "a191", "a192", "a193", "a194", "a195", "a196", "a197", "a198", "a199", "a200", "a201", "a202", "a203", "a204", "a205", "a206", "a207", "a208", "a209", "a210", "a211", "a212", "a213", "a214", "a215", "a216", "a217", "a218", "a219", "a220", "a221", "a222", "a223", "a224", "a225", "a226", "a227", "a228", "a229", "a230", "a231", "a232", "a233", "a234", "a235", "a236", "a237", "a238", "a239", "a240", "a241", "a242", "a243", "a244", "a245", "a246", "a247", "a248", "a249", "a250", "a251", "a252", "a253", "a254", "a255", "s4", "s5", "s6", "s7", "s8", "s9", "s10", "s11", "s12", "s13", "s14", "s15", "s16", "s17", "s18", "s19", "s20", "s21", "s22", "s23", "s24", "s25", "s26", "s27", "s28", "s29", "s30", "s31", "s32", "s33", "s34", "s35", "s36", "s37", "s38", "s39", "s40", "s41", "s42", "s43", "s44", "s45", "s46", "s47", "s48", "s49", "s50", "s51", "s52", "s53", "s54", "s55", "s56", "s57", "s58", "s59", "s60", "s61", "s62", "s63", "s64", "s65", "s66", "s67", "s68", "s69", "s70", "s71", "s72", "s73", "s74", "s75", "s76", "s77", "s78", "s79", "s80", "s81", "s82", "s83", "s84", "s85", "s86", "s87", "s88", "s89", "s90", "s91", "s92", "s93", "s94", "s95", "s96", "s97", "s98", "s99");
}

static inline U desc(uint64_t bytes) { return ((U)0x20000 << 32) | bytes; }

extern "C" void kernel_launch(void* const* d_in, const int* in_sizes, int n_in, void* d_out, int out_size, void* d_ws, size_t ws_size, hipStream_t stream) {
  (void)hipFuncSetAttribute((const void*)fused_kernel, hipFuncAttributeMaxDynamicSharedMemorySize, LDS_TOTAL);
  const U big = desc(8u << 20), half = desc(4u << 20), tab = desc(512u << 10), sc = desc(32u << 10);
  fused_kernel<<<256, 256, LDS_TOTAL, stream>>>(d_out, half, (char*)d_ws + 0x70000, desc(WS_BYTES - 0x70000), d_in[1], big, d_in[4], big, d_in[9], tab, d_in[10], tab, d_in[2], sc, d_in[3], sc,
                                                d_in[5], sc, d_in[6], sc, d_in[0], half, d_in[7], tab, d_in[8], tab, d_ws, desc(WS_BYTES));
}
```
